```python
import torch

def _softmax(x, axis=-1):
    return torch.softmax(x, dim=axis)
def _gelu(x):
    return torch.nn.functional.gelu(x, approximate="tanh")
def _silu(x):
    return torch.nn.functional.silu(x)
def _sigmoid(x):
    return torch.sigmoid(x)
def _layernorm(x, axis=-1, eps=1e-5):
    m = x.mean(dim=axis, keepdim=True)
    v = x.var(dim=axis, keepdim=True, unbiased=False)
    return (x - m) / torch.sqrt(v + eps)
def _scatter_add(tgt, idx, src, axis=0):
    return tgt.clone().index_add_(axis, idx, src)
def _segment_sum(src, seg, n):
    out = torch.zeros((n,) + src.shape[1:], dtype=src.dtype)
    return out.index_add_(0, seg, src)
def _pad(x, pw):
    flat = tuple(v for pair in reversed(pw) for v in pair)
    return torch.nn.functional.pad(x, flat)

kernel_name = 'parallel_sum__softmax_full_mha__dense_activation_gelu__prompt'

def setup_inputs():

    _w = (lambda a: (lambda t: t.to(torch.bfloat16) if t.is_floating_point() else t)(torch.as_tensor(a)))
    def _A_setup():
        B, S, D = 1, 2048, 512
        return {
            "x": _w((lambda *s: torch.randn(*s, dtype=torch.bfloat16) * 0.05)(B, S, D)),
            "wqkv": _w((lambda *s: torch.randn(*s, dtype=torch.bfloat16) * 0.05)(D, 3 * D)),
            "wo": _w((lambda *s: torch.randn(*s, dtype=torch.bfloat16) * 0.05)(D, D)),
        }
    _A_in = _A_setup()

    def _B_setup():
        B, S, D, F = 1, 2048, 512, 1792
        return {
            "x": _w((lambda *s: torch.randn(*s, dtype=torch.bfloat16) * 0.05)(B, S, D)),
            "w1": _w((lambda *s: torch.randn(*s, dtype=torch.bfloat16) * 0.8)(D, F) * (D ** -0.5)),
            "b1": _w((lambda *s: torch.randn(*s, dtype=torch.bfloat16) * 12.8)(F) * 0.01),
            "w2": _w((lambda *s: torch.randn(*s, dtype=torch.bfloat16) * 0.8)(F, D) * (F ** -0.5)),
            "b2": _w((lambda *s: torch.randn(*s, dtype=torch.bfloat16) * 12.8)(D) * 0.01),
        }
    _B_in = _B_setup()

    return {
        "A_x": _A_in["x"],
        "A_wqkv": _A_in["wqkv"],
        "A_wo": _A_in["wo"],
        "B_x": _B_in["x"],
        "B_w1": _B_in["w1"],
        "B_b1": _B_in["b1"],
        "B_w2": _B_in["w2"],
        "B_b2": _B_in["b2"],
    }

def reference(A_x, A_wqkv, A_wo, B_x, B_w1, B_b1, B_w2, B_b2):
    def _A_ref(x, wqkv, wo):
        B, S, D = x.shape
        H = 32; Dh = D // H
        qkv = (lambda x, *d: x.permute(*d))((x @ wqkv).reshape(B, S, 3, H, Dh), 2, 0, 3, 1, 4)
        q, k, v = qkv[0], qkv[1], qkv[2]
        scale = Dh ** -0.5
        scores = torch.einsum("bhqd,bhkd->bhqk", q, k) * scale
        p = _softmax(scores, axis=-1)
        o = torch.einsum("bhqk,bhkd->bhqd", p, v)
        return (lambda x, *d: x.permute(*d))(o, 0, 2, 1, 3).reshape(B, S, D) @ wo

    def _B_ref(x, w1, b1, w2, b2):
        h = torch.einsum("bsd,df->bsf", x, w1) + b1
        h = _gelu(h)
        return torch.einsum("bsf,fd->bsd", h, w2) + b2

    x = A_x
    xn = _layernorm(x, axis=-1)
    return x + _A_ref(xn, A_wqkv, A_wo) + _B_ref(xn, B_w1, B_b1, B_w2, B_b2)
```

```cpp
#include <hip/hip_runtime.h>
#include <hip/hip_bf16.h>
#include <stdint.h>

typedef __attribute__((ext_vector_type(8))) __bf16 bf16x8;
typedef __attribute__((ext_vector_type(4))) __bf16 bf16x4;
typedef __attribute__((ext_vector_type(2))) __bf16 bf16x2;
typedef __attribute__((ext_vector_type(2))) float f32x2;
typedef __attribute__((ext_vector_type(4))) float f32x4;
typedef __attribute__((ext_vector_type(16))) float f32x16;
typedef __attribute__((ext_vector_type(4))) unsigned u32x4;
typedef __attribute__((ext_vector_type(2))) unsigned u32x2;
typedef unsigned short u16;
#define LDSP __attribute__((address_space(3)))
#define DI __device__ __forceinline__
#define MFMA(a, b, c) __builtin_amdgcn_mfma_f32_32x32x16_bf16((a), (b), (c), 0, 0, 0)

constexpr int S_ = 2048, D_ = 512, F_ = 1792;
constexpr size_t WS_Q = 0;
constexpr size_t WS_KV = 2u << 20;
constexpr size_t WS_H = 6u << 20;
constexpr size_t WS_O = 14u << 20;
constexpr size_t WS_Y = 16u << 20;
constexpr int LDS_BYTES = 163840;

DI void dma16(const void* sbase, unsigned voff, unsigned lds_dst) {
  unsigned keep;
  asm volatile("s_mov_b32 %0, m0\n\ts_mov_b32 m0, %3\n\ts_nop 0\n\tglobal_load_lds_dwordx4 %1, %2\n\ts_mov_b32 m0, %0"
               : "=&s"(keep) : "v"(voff), "s"(sbase), "s"(lds_dst) : "memory");
}
#define VMCNT(n) asm volatile("s_waitcnt vmcnt(" #n ")" ::: "memory")
#define LGKM0() asm volatile("s_waitcnt lgkmcnt(0)" ::: "memory")
#define BAR() do { asm volatile("s_waitcnt lgkmcnt(0)\n\ts_barrier" ::: "memory"); } while (0)

DI bf16x8 lds_b128(unsigned a) { return *(LDSP bf16x8*)(uintptr_t)a; }
DI f32x4 lds_f4(unsigned a) { return *(LDSP f32x4*)(uintptr_t)a; }
DI float lds_f1(unsigned a) { return *(LDSP float*)(uintptr_t)a; }
DI void lds_wf1(unsigned a, float v) { *(LDSP float*)(uintptr_t)a = v; }
DI void lds_wf4(unsigned a, f32x4 v) { *(LDSP f32x4*)(uintptr_t)a = v; }
DI bf16x8 lds_tr2(unsigned a, unsigned d) {
  bf16x4 lo = __builtin_amdgcn_ds_read_tr16_b64_v4bf16((LDSP bf16x4*)(uintptr_t)a);
  bf16x4 hi = __builtin_amdgcn_ds_read_tr16_b64_v4bf16((LDSP bf16x4*)(uintptr_t)(a + d));
  return __builtin_shufflevector(lo, hi, 0, 1, 2, 3, 4, 5, 6, 7);
}
DI float dot2(bf16x2 a, bf16x2 b, float c) { return __builtin_amdgcn_fdot2_f32_bf16(a, b, c, false); }
DI float frag_sum(bf16x8 f, float c) {
  const bf16x2 one = {(__bf16)1.0f, (__bf16)1.0f};
  c = dot2(__builtin_shufflevector(f, f, 0, 1), one, c); c = dot2(__builtin_shufflevector(f, f, 2, 3), one, c);
  c = dot2(__builtin_shufflevector(f, f, 4, 5), one, c); c = dot2(__builtin_shufflevector(f, f, 6, 7), one, c);
  return c;
}
DI float frag_sq(bf16x8 f, float c) {
  bf16x2 a = __builtin_shufflevector(f, f, 0, 1), b = __builtin_shufflevector(f, f, 2, 3), d = __builtin_shufflevector(f, f, 4, 5), e = __builtin_shufflevector(f, f, 6, 7);
  c = dot2(a, a, c); c = dot2(b, b, c); c = dot2(d, d, c); c = dot2(e, e, c);
  return c;
}
DI unsigned pk2(float a, float b) { f32x2 v = {a, b}; bf16x2 c = __builtin_convertvector(v, bf16x2); return __builtin_bit_cast(unsigned, c); }
DI float rbf(float a) { return __uint_as_float(pk2(a, a) << 16); }
DI float b2f(u16 b) { return __uint_as_float((unsigned)b << 16); }
DI float half_sum(float v) {
  unsigned u = __float_as_uint(v);
  auto r = __builtin_amdgcn_permlane32_swap(u, u, false, false);
  unsigned r0 = r[0], r1 = r[1];
  return __uint_as_float(r0) + __uint_as_float(r1);
}
DI float half_max(float v) {
  unsigned u = __float_as_uint(v);
  auto r = __builtin_amdgcn_permlane32_swap(u, u, false, false);
  unsigned r0 = r[0], r1 = r[1];
  return fmaxf(__uint_as_float(r0), __uint_as_float(r1));
}
DI float gelu(float x) {
  const float c1 = -2.302208198f, c2 = -0.1029432397f;
  float z = x * __builtin_fmaf(x * x, c2, c1);
  float e = __builtin_amdgcn_exp2f(z);
  return x * __builtin_amdgcn_rcpf(1.0f + e);
}

constexpr unsigned SL0 = 0, SL1 = 32768, SL2 = 65536, SL3 = 98304, SL4 = 131072;
constexpr unsigned SCR = SL4;
constexpr unsigned SCR_RA = SCR, SCR_RB = SCR + 512, SCR_CS = SCR + 1024  , SCR_PV = SCR + 1024 + 3584  ;

template <bool T_ORIENT, int NB>
DI void kloop(const bf16x8 (&xf)[32], int s0, const unsigned (&wb)[4], unsigned slot0, unsigned slot1, f32x16& a0, f32x16& a1, float& c0, float& c1) {
#pragma unroll
  for (int i = 0; i < 16; i++) {
    const int s = s0 + i;
    const unsigned ad = wb[s & 3] + (s >> 2) * 4096;
    bf16x8 w0 = lds_tr2(ad + slot0, 256);
    a0 = T_ORIENT ? MFMA(w0, xf[s], a0) : MFMA(xf[s], w0, a0);
    if ((s & 3) == 0) c0 = frag_sum(w0, c0);
    if (NB == 2) {
      bf16x8 w1 = lds_tr2(ad + slot1, 256);
      a1 = MFMA(w1, xf[s], a1);
      if ((s & 3) == 0) c1 = frag_sum(w1, c1);
    }
  }
}

__global__ __launch_bounds__(512, 2) void k_phase1(const u16* __restrict__ x, const u16* __restrict__ wqkv, const u16* __restrict__ w1,
                                                   const u16* __restrict__ b1, char* __restrict__ ws) {
  const int tid = threadIdx.x, lane = tid & 63, w = __builtin_amdgcn_readfirstlane(tid >> 6), m = w & 3, hf = w >> 2;
  const int b = blockIdx.x, xc = b & 7, idx = b >> 3;
  const int rb = 4 * (xc >> 1) + (idx & 3), cg = 8 * (xc & 1) + (idx >> 2);
  const int nfb = (cg & 1) ? 3 : 4, fb0 = 7 * (cg >> 1) + ((cg & 1) ? 4 : 0);
  const int t = lane & 31, h = lane >> 5;

  float bias = 0.f;
  if (tid < nfb * 32) bias = b2f(b1[fb0 * 32 + tid]);
  VMCNT(0);

  const char* xb = (const char*)x + (size_t)(rb * 128 + 16 * w) * 1024;
  unsigned xo[8];
#pragma unroll
  for (int i = 0; i < 8; i++) { const int r = 2 * i + h; xo[i] = r * 1024 + ((t ^ (r & 15)) << 4); }
#pragma unroll
  for (int i = 0; i < 8; i++) dma16(xb, xo[i], SL0 + (16 * w + 2 * i) * 512);
  const unsigned wo_q = (lane >> 2) * 3072 + (lane & 3) * 16;
  const char* wqb = (const char*)wqkv + cg * 64;
#pragma unroll
  for (int hh = 0; hh < 2; hh++)
#pragma unroll
    for (int j = 0; j < 6; j++) {
      const int blk = j >> 1, r0 = hh * 256 + (2 * w + (j & 1)) * 16;
      dma16(wqb + blk * 1024 + (size_t)r0 * 3072, wo_q, SL2 + blk * 32768 + r0 * 64);
    }
  unsigned xa[8];
#pragma unroll
  for (int i = 0; i < 8; i++) xa[i] = (32 * m + t) * 512 + ((((i ^ m) << 1) ^ h ^ (t & 15)) << 4);
  unsigned wb[4];
  {
    const int g = lane >> 4, q = (lane & 15) >> 2, p = lane & 3;
#pragma unroll
    for (int i = 0; i < 4; i++) wb[i] = ((i ^ m) * 16 + 8 * (g >> 1) + q) * 64 + (g & 1) * 32 + p * 8;
  }
  bf16x8 xf[32];
  VMCNT(12); BAR();
#pragma unroll
  for (int i = 0; i < 16; i++) xf[i] = lds_b128(xa[i & 7] + (i >> 3) * 256);
  VMCNT(6); BAR();
#pragma unroll
  for (int i = 0; i < 8; i++) dma16(xb + 512, xo[i], SL0 + (16 * w + 2 * i) * 512);

  f32x16 a0 = {}, a1 = {};
  float c0 = 0.f, c1 = 0.f, rs = 0.f, rq = 0.f;
  if (hf == 0) kloop<true, 2>(xf, 0, wb, SL2, SL3, a0, a1, c0, c1);
  else {
    kloop<false, 1>(xf, 0, wb, SL4, 0, a0, a1, c0, c1);
#pragma unroll
    for (int i = 0; i < 16; i++) { rs = frag_sum(xf[i], rs); rq = frag_sq(xf[i], rq); }
  }
  VMCNT(0); BAR();
#pragma unroll
  for (int i = 0; i < 16; i++) xf[16 + i] = lds_b128(xa[i & 7] + (i >> 3) * 256);
  BAR();
  const unsigned wo_1 = (lane >> 2) * 3584 + (lane & 3) * 16;
  const char* w1b = (const char*)w1 + fb0 * 64;
#pragma unroll
  for (int j = 0; j < 8; j++) {
    const int blk = j >> 2, r0 = (4 * w + (j & 3)) * 16;
    dma16(w1b + blk * 64 + (size_t)r0 * 3584, wo_1, SL0 + blk * 32768 + r0 * 64);
  }
#pragma unroll
  for (int j = 0; j < 4; j++) {
    const int blk = 2 + (j >> 1), r0 = (2 * w + (j & 1)) * 16;
    dma16(w1b + (blk < nfb ? blk : 0) * 64 + (size_t)r0 * 3584, wo_1, SL0 + blk * 32768 + r0 * 64);
  }
  if (hf == 0) kloop<true, 2>(xf, 16, wb, SL2, SL3, a0, a1, c0, c1);
  else {
    kloop<false, 1>(xf, 16, wb, SL4, 0, a0, a1, c0, c1);
#pragma unroll
    for (int i = 16; i < 32; i++) { rs = frag_sum(xf[i], rs); rq = frag_sq(xf[i], rq); }
    rs = half_sum(rs); rq = half_sum(rq);
    const float mean = rs * (1.0f / 512), var = fmaxf(rq * (1.0f / 512) - mean * mean, 0.f);
    const float den = rbf(__builtin_amdgcn_sqrtf(rbf(rbf(var) + 1e-5f)));
    const float ra = 1.0f / den;
    if (h == 0) { lds_wf1(SCR_RA + (32 * m + t) * 4, ra); lds_wf1(SCR_RB + (32 * m + t) * 4, -rbf(mean) * ra); }
  }
  c0 = half_sum(c0); c1 = half_sum(c1);
  if (h == 0) {
    lds_wf1(SCR_CS + ((hf == 0 ? 0 : 2) * 32 + t) * 16 + m * 4, c0);
    if (hf == 0) lds_wf1(SCR_CS + (32 + t) * 16 + m * 4, c1);
  }
  if (tid < 128) lds_wf1(SCR_PV + 2048 + tid * 4, bias);
  VMCNT(4); BAR();
#pragma unroll
  for (int j = 0; j < 4; j++) {
    const int blk = 2 + (j >> 1), r0 = 256 + (2 * w + (j & 1)) * 16;
    dma16(w1b + (blk < nfb ? blk : 0) * 64 + (size_t)r0 * 3584, wo_1, SL0 + blk * 32768 + r0 * 64);
  }
  const float ra = lds_f1(SCR_RA + (32 * m + t) * 4), rbv = lds_f1(SCR_RB + (32 * m + t) * 4);
  const int tile = rb * 4 + m;
  const unsigned pv = SCR_PV + w * 256;
  if (hf == 0) {
    f32x4 p0 = lds_f4(SCR_CS + t * 16), p1 = lds_f4(SCR_CS + (32 + t) * 16);
    if (h == 0) { lds_wf1(pv + t * 4, (p0[0] + p0[1]) + (p0[2] + p0[3])); lds_wf1(pv + 128 + t * 4, (p1[0] + p1[1]) + (p1[2] + p1[3])); }
    const float qs = 0.25f * 1.4426950408889634f;
    u32x4 o[4];
#pragma unroll
    for (int g = 0; g < 4; g++) {
      f32x4 cq = lds_f4(pv + (8 * g + 4 * h) * 4), ck = lds_f4(pv + 128 + (8 * g + 4 * h) * 4);
      float vq[4], vk[4];
#pragma unroll
      for (int j = 0; j < 4; j++) { vq[j] = __builtin_fmaf(ra, a0[4 * g + j], rbv * cq[j]) * qs; vk[j] = __builtin_fmaf(ra, a1[4 * g + j], rbv * ck[j]); }
      o[g >> 1][(g & 1) * 2] = pk2(vq[0], vq[1]); o[g >> 1][(g & 1) * 2 + 1] = pk2(vq[2], vq[3]);
      o[2 + (g >> 1)][(g & 1) * 2] = pk2(vk[0], vk[1]); o[2 + (g >> 1)][(g & 1) * 2 + 1] = pk2(vk[2], vk[3]);
    }
#pragma unroll
    for (int e = 0; e < 2; e++) {
      *(u32x4*)(ws + WS_Q + (size_t)((2 * cg + e) * 64 + tile) * 1024 + lane * 16) = o[e];
      *(u32x4*)(ws + WS_KV + (size_t)((2 * cg + e) * 64 + tile) * 2048 + lane * 16) = o[2 + e];
    }
  } else {
    f32x4 p0 = lds_f4(SCR_CS + (64 + t) * 16);
    const float cv = (p0[0] + p0[1]) + (p0[2] + p0[3]);
    u32x4 o[2];
#pragma unroll
    for (int g = 0; g < 4; g++) {
      f32x4 ta = lds_f4(SCR_RA + (32 * m + 8 * g + 4 * h) * 4), tb = lds_f4(SCR_RB + (32 * m + 8 * g + 4 * h) * 4);
      float v[4];
#pragma unroll
      for (int j = 0; j < 4; j++) v[j] = __builtin_fmaf(ta[j], a0[4 * g + j], tb[j] * cv);
      o[g >> 1][(g & 1) * 2] = pk2(v[0], v[1]); o[g >> 1][(g & 1) * 2 + 1] = pk2(v[2], v[3]);
    }
#pragma unroll
    for (int e = 0; e < 2; e++)
      *(u32x4*)(ws + WS_KV + (size_t)((2 * cg + (t >> 4)) * 64 + tile) * 2048 + 1024 + e * 512 + h * 256 + (t & 15) * 16) = o[e];
  }
#pragma unroll
  for (int r = 0; r < 2; r++) {
    const int blk = 2 * r + hf;
    f32x16 a = {}, dum = {};
    float c = 0.f, cd = 0.f;
    const unsigned slot = SL0 + blk * 32768;
    unsigned wb2[4];
#pragma unroll
    for (int i = 0; i < 4; i++) wb2[i] = wb[i] + slot;
    if (blk < nfb) {
      kloop<true, 1>(xf, 0, wb2, 0, 0, a, dum, c, cd);
      kloop<true, 1>(xf, 16, wb2, 0, 0, a, dum, c, cd);
      c = half_sum(c);
      if (h == 0) lds_wf1(SCR_CS + ((3 + blk) * 32 + t) * 16 + m * 4, c);
    }
    VMCNT(0); BAR();
    if (blk < nfb) {
      f32x4 p0 = lds_f4(SCR_CS + ((3 + blk) * 32 + t) * 16);
      if (h == 0) lds_wf1(pv + t * 4, (p0[0] + p0[1]) + (p0[2] + p0[3]));
      u32x4 o[2];
#pragma unroll
      for (int g = 0; g < 4; g++) {
        f32x4 cs = lds_f4(pv + (8 * g + 4 * h) * 4), bs = lds_f4(SCR_PV + 2048 + (blk * 32 + 8 * g + 4 * h) * 4);
        float v[4];
#pragma unroll
        for (int j = 0; j < 4; j++) v[j] = gelu(__builtin_fmaf(ra, a[4 * g + j], __builtin_fmaf(rbv, cs[j], bs[j])));
        o[g >> 1][(g & 1) * 2] = pk2(v[0], v[1]); o[g >> 1][(g & 1) * 2 + 1] = pk2(v[2], v[3]);
      }
#pragma unroll
      for (int e = 0; e < 2; e++)
        *(u32x4*)(ws + WS_H + (size_t)(tile * 112 + 2 * (fb0 + blk) + e) * 1024 + lane * 16) = o[e];
    }
  }
}

constexpr unsigned KV0 = 0, MISC = 131072, RING = MISC + 8192;

#define R_QA "v[128:131]"
#define R_QB "v[132:135]"
#define ATT_MACROS \
  ".macro AT_PHASE sx, sy, ox, oy, qy, ny, w1, l1, w2, l2, w3, l3\n" \
  "v_exp_f32 v[\\sx+0], v[\\sx+0]\n" \
  "v_cvt_pk_bf16_f32 v[207], v[\\sy+14], v[\\sy+15]\n" \
  "v_exp_f32 v[\\sx+1], v[\\sx+1]\n" \
  "\\w1\n" \
  "v_exp_f32 v[\\sx+2], v[\\sx+2]\n" \
  "v_mfma_f32_32x32x16_bf16 v[\\oy:\\oy+15], v[248:251], v[204:207], v[\\oy:\\oy+15]\n" \
  "v_cvt_pk_bf16_f32 v[200], v[\\sx+0], v[\\sx+1]\n" \
  "v_exp_f32 v[\\sx+3], v[\\sx+3]\n" \
  "\\l1\n" \
  "v_exp_f32 v[\\sx+4], v[\\sx+4]\n" \
  "v_cvt_pk_bf16_f32 v[201], v[\\sx+2], v[\\sx+3]\n" \
  "v_exp_f32 v[\\sx+5], v[\\sx+5]\n" \
  "v_exp_f32 v[\\sx+6], v[\\sx+6]\n" \
  "\\w2\n" \
  "v_cvt_pk_bf16_f32 v[202], v[\\sx+4], v[\\sx+5]\n" \
  "v_mfma_f32_32x32x16_bf16 v[\\sy:\\sy+15], v[240:243], v[\\qy:\\qy+3], v[\\ny:\\ny+15]\n" \
  "v_exp_f32 v[\\sx+7], v[\\sx+7]\n" \
  "\\l2\n" \
  "v_exp_f32 v[\\sx+8], v[\\sx+8]\n" \
  "v_cvt_pk_bf16_f32 v[203], v[\\sx+6], v[\\sx+7]\n" \
  "v_exp_f32 v[\\sx+9], v[\\sx+9]\n" \
  "v_exp_f32 v[\\sx+10], v[\\sx+10]\n" \
  "v_cvt_pk_bf16_f32 v[204], v[\\sx+8], v[\\sx+9]\n" \
  "v_exp_f32 v[\\sx+11], v[\\sx+11]\n" \
  "\\w3\n" \
  "v_mfma_f32_32x32x16_bf16 v[\\ox:\\ox+15], v[244:247], v[200:203], v[\\ox:\\ox+15]\n" \
  "v_exp_f32 v[\\sx+12], v[\\sx+12]\n" \
  "\\l3\n" \
  "v_cvt_pk_bf16_f32 v[205], v[\\sx+10], v[\\sx+11]\n" \
  "v_exp_f32 v[\\sx+13], v[\\sx+13]\n" \
  "v_exp_f32 v[\\sx+14], v[\\sx+14]\n" \
  "v_cvt_pk_bf16_f32 v[206], v[\\sx+12], v[\\sx+13]\n" \
  "v_exp_f32 v[\\sx+15], v[\\sx+15]\n" \
  ".endm\n"

#define AT_TILE(ko, v1o, upd, v0o) \
  "AT_PHASE 168, 184, 208, 224, 132, 152, \"\", \"ds_read_b128 v[248:251], v253 offset:" #v1o "\", \"\", \"ds_read_b128 v[240:243], v252 offset:" #ko "\", \"s_waitcnt lgkmcnt(2)\", \"\"\n" \
  upd \
  "AT_PHASE 184, 168, 224, 208, 128, 136, \"s_waitcnt lgkmcnt(1)\", \"\", \"s_waitcnt lgkmcnt(0)\", \"\", \"\", \"ds_read_b128 v[244:247], v253 offset:" #v0o "\"\n"

DI void attn_loop(bf16x8 qa, bf16x8 qb, const f32x16& na, const f32x16& nb, unsigned ka, unsigned va, unsigned vs, f32x16& oa, f32x16& ob) {
  asm volatile(
      ATT_MACROS
      "ds_read_b128 v[240:243], v252\n"
      "ds_read_b128 v[244:247], v253\n"
      "v_mov_b32 v204, 0\n v_mov_b32 v205, 0\n v_mov_b32 v206, 0\n v_mov_b32 v198, 0\n v_mov_b32 v199, 0\n"
      "v_mov_b32 v248, 0\n v_mov_b32 v249, 0\n v_mov_b32 v250, 0\n v_mov_b32 v251, 0\n"
      "s_mov_b32 s40, 16\n"
      "s_waitcnt lgkmcnt(1)\n"
      "v_mfma_f32_32x32x16_bf16 v[168:183], v[240:243], v[128:131], v[136:151]\n"
      "s_nop 15\n"
      "1:\n"
      AT_TILE(2048, 512, "", 2048)
      AT_TILE(4096, 2560, "", 4096)
      AT_TILE(6144, 4608, "", 6144)
      AT_TILE(8192, 6656, "v_add_u32 v252, 0x2000, v252\n s_sub_u32 s40, s40, 1\n v_add_u32 v253, v254, v253\n s_cmp_lg_u32 s40, 0\n", 0)
      "s_cbranch_scc1 1b\n"
      "s_nop 0\n"
      "v_cvt_pk_bf16_f32 v207, v198, v199\n"
      "s_nop 1\n"
      "v_mfma_f32_32x32x16_bf16 v[224:239], v[248:251], v[204:207], v[224:239]\n"
      "s_waitcnt lgkmcnt(0)\n"
      "s_nop 15\n"
      ".purgem AT_PHASE\n"
      : "+{v[208:223]}"(oa), "+{v[224:239]}"(ob), "+{v252}"(ka), "+{v253}"(va)
      : "{v[128:131]}"(qa), "{v[132:135]}"(qb), "{v[136:151]}"(na), "{v[152:167]}"(nb), "{v254}"(vs)
      : "v168", "v169", "v170", "v171", "v172", "v173", "v174", "v175", "v176", "v177", "v178", "v179", "v180", "v181", "v182", "v183",
        "v184", "v185", "v186", "v187", "v188", "v189", "v190", "v191", "v192", "v193", "v194", "v195", "v196", "v197", "v198", "v199",
        "v200", "v201", "v202", "v203", "v204", "v205", "v206", "v207", "v240", "v241", "v242", "v243", "v244", "v245", "v246", "v247",
        "v248", "v249", "v250", "v251", "s40", "scc", "memory");
}

DI float max16(const f32x16& s, float m) {
#pragma unroll
  for (int i = 0; i < 16; i += 2) m = fmaxf(fmaxf(s[i], s[i + 1]), m);
  return m;
}

DI void gload(u32x4& d, const void* sbase, unsigned voff) { asm volatile("global_load_dwordx4 %0, %1, %2" : "=v"(d) : "v"(voff), "s"(sbase) : "memory"); }

__global__ __launch_bounds__(512, 2) void k_phase2(const u16* __restrict__ w2, char* __restrict__ ws) {
  const int tid = threadIdx.x, lane = tid & 63, w = __builtin_amdgcn_readfirstlane(tid >> 6);
  const int b = blockIdx.x, xc = b & 7, idx = b >> 3;
  const int head = 4 * xc + (idx & 3), qb = idx >> 2;
  const int rt = 4 * xc + (idx & 3), ct = idx >> 2;
  const int t = lane & 31, h = lane >> 5;
  const char* kv = ws + WS_KV + (size_t)head * 131072;
#pragma unroll
  for (int i = 0; i < 16; i++) dma16(kv + (i * 8 + w) * 1024, lane * 16, KV0 + (i * 8 + w) * 1024);
  if (tid < 8) { u32x4 ones = {0x3f803f80u, 0x3f803f80u, 0x3f803f80u, 0x3f803f80u}; *(LDSP u32x4*)(uintptr_t)(MISC + (tid >> 1) * 2048 + (tid & 1) * 512) = ones; }
  f32x16 acc[4] = {};
  if (w < 4) {
    const char* qp = ws + WS_Q + (size_t)(head * 64 + qb * 8 + 2 * w) * 1024 + lane * 16;
    const bf16x8 qa = *(const bf16x8*)qp, qbf = *(const bf16x8*)(qp + 1024);
    VMCNT(0); BAR();
    const unsigned ka = KV0 + lane * 16;
    const unsigned va = t < 16 ? KV0 + 1024 + h * 256 + t * 16 : MISC, vs = t < 16 ? 8192u : 0u;
    int nmax = 1;
    f32x16 oa, ob;
    for (;;) {
      float ma = -3.0e38f, mb = -3.0e38f;
      for (int i = 0; i < nmax; i++) {
        const bf16x8 kf = lds_b128(ka + i * 2048);
        const f32x16 z = {};
        ma = max16(MFMA(kf, qa, z), ma); mb = max16(MFMA(kf, qbf, z), mb);
      }
      ma = half_max(ma); mb = half_max(mb);
      f32x16 na, nb;
#pragma unroll
      for (int i = 0; i < 16; i++) { na[i] = -ma; nb[i] = -mb; oa[i] = 0.f; ob[i] = 0.f; }
      attn_loop(qa, qbf, na, nb, ka, va, vs, oa, ob);
      const bool ok = oa[8] < 1.0e12f && ob[8] < 1.0e12f;
      if (nmax != 1 || __all(ok)) break;
      nmax = 64;
    }
    const float ia = __builtin_amdgcn_rcpf(oa[8]), ib = __builtin_amdgcn_rcpf(ob[8]);
    u32x4 o0, o1;
#pragma unroll
    for (int j = 0; j < 4; j++) { o0[j] = pk2(oa[2 * j] * ia, oa[2 * j + 1] * ia); o1[j] = pk2(ob[2 * j] * ib, ob[2 * j + 1] * ib); }
    char* op = ws + WS_O + (size_t)((qb * 8 + 2 * w) * 32 + head) * 1024 + lane * 16;
    *(u32x4*)op = o0; *(u32x4*)(op + 32768) = o1;
  } else {
    VMCNT(0); BAR();
    const int g = w - 4;
    const char* hp = ws + WS_H + (size_t)(2 * rt * 112 + 28 * g) * 1024;
    const char* wp = (const char*)w2 + (size_t)(28 * g * 16) * 1024 + ct * 128;
    const unsigned wvo = (lane >> 3) * 1024 + (lane & 7) * 16, hvo = lane * 16, hvo2 = lane * 16 + 112 * 1024;
    const unsigned ring = RING + g * 6144;
    const unsigned tr0 = ring + (4 * (lane >> 5) + ((lane & 15) >> 2)) * 128 + ((lane >> 4) & 1) * 32 + (lane & 3) * 8;
    u32x4 hb[4][2];
#pragma unroll
    for (int s = 0; s < 3; s++) {
      dma16(wp + s * 16384, wvo, ring + s * 2048); dma16(wp + s * 16384 + 8192, wvo, ring + s * 2048 + 1024);
      gload(hb[s][0], hp + s * 1024, hvo); gload(hb[s][1], hp + s * 1024, hvo2);
    }
    gload(hb[3][0], hp + 3 * 1024, hvo); gload(hb[3][1], hp + 3 * 1024, hvo2);
    unsigned slot = 0;
    for (int it = 0; it < 7; it++) {
#pragma unroll
      for (int u = 0; u < 4; u++) {
        const int s = it * 4 + u;
        asm volatile("s_waitcnt vmcnt(10)" : "+v"(hb[u][0]), "+v"(hb[u][1]) :: "memory");
        const bf16x8 wa = lds_tr2(tr0 + slot, 1024), wb = lds_tr2(tr0 + slot + 64, 1024);
        const bf16x8 h0 = __builtin_bit_cast(bf16x8, hb[u][0]), h1 = __builtin_bit_cast(bf16x8, hb[u][1]);
        acc[0] = MFMA(wa, h0, acc[0]); acc[1] = MFMA(wb, h0, acc[1]); acc[2] = MFMA(wa, h1, acc[2]); acc[3] = MFMA(wb, h1, acc[3]);
        asm volatile("" : "+v"(acc[0]), "+v"(acc[1]), "+v"(acc[2]), "+v"(acc[3]));
        const int sw = s + 3 < 28 ? s + 3 : 27, sh = s + 4 < 28 ? s + 4 : 27;
        LGKM0();
        dma16(wp + sw * 16384, wvo, ring + slot); dma16(wp + sw * 16384 + 8192, wvo, ring + slot + 1024);
        gload(hb[u][0], hp + sh * 1024, hvo); gload(hb[u][1], hp + sh * 1024, hvo2);
        slot = slot == 4096 ? 0 : slot + 2048;
      }
    }
    asm volatile("s_waitcnt vmcnt(0)" : "+v"(hb[0][0]), "+v"(hb[0][1]), "+v"(hb[1][0]), "+v"(hb[1][1]), "+v"(hb[2][0]), "+v"(hb[2][1]), "+v"(hb[3][0]), "+v"(hb[3][1]) :: "memory");
  }
  BAR();
  if (w >= 4) {
    const unsigned base = (w - 4) * (64 * 272);
#pragma unroll
    for (int a = 0; a < 4; a++)
#pragma unroll
      for (int g = 0; g < 4; g++) {
        f32x4 v = {acc[a][4 * g], acc[a][4 * g + 1], acc[a][4 * g + 2], acc[a][4 * g + 3]};
        lds_wf4(base + ((a >> 1) * 32 + t) * 272 + ((a & 1) * 32 + 8 * g + 4 * h) * 4, v);
      }
  }
  BAR();
  {
    const int r = tid >> 3, c = (tid & 7) * 8;
    f32x4 s0 = {}, s1 = {};
#pragma unroll
    for (int g = 0; g < 4; g++) { s0 += lds_f4(g * (64 * 272) + r * 272 + c * 4); s1 += lds_f4(g * (64 * 272) + r * 272 + c * 4 + 16); }
    float* yp = (float*)(ws + WS_Y) + (size_t)(rt * 64 + r) * 512 + ct * 64 + c;
    *(f32x4*)yp = s0; *(f32x4*)(yp + 4) = s1;
  }
}

__global__ __launch_bounds__(256) void k_phase3(const u16* __restrict__ x, const u16* __restrict__ wo, const u16* __restrict__ b2,
                                                const char* __restrict__ ws, u16* __restrict__ out) {
  const int tid = threadIdx.x, lane = tid & 63, g = __builtin_amdgcn_readfirstlane(tid >> 6);
  const int b = blockIdx.x, xc = b & 7, idx = b >> 3;
  const int rt = 4 * xc + (idx & 3), ct = idx >> 2;
  const int t = lane & 31, h = lane >> 5;
  const char* wp = (const char*)wo + (size_t)(8 * g * 16) * 1024 + ct * 128;
  const unsigned wvo = (lane >> 3) * 1024 + (lane & 7) * 16;
  const unsigned ring = 69632 + g * 16384;
#pragma unroll
  for (int s = 0; s < 16; s++) dma16(wp + s * 8192, wvo, ring + s * 1024);
  const char* op = ws + WS_O + (size_t)(2 * rt * 32 + 8 * g) * 1024 + lane * 16;
  bf16x8 of[8][2];
#pragma unroll
  for (int s = 0; s < 8; s++) { of[s][0] = *(const bf16x8*)(op + s * 1024); of[s][1] = *(const bf16x8*)(op + s * 1024 + 32768); }
  const int r = tid >> 2, c = (tid & 3) * 16;
  const float* yp = (const float*)(ws + WS_Y) + (size_t)(rt * 64 + r) * 512 + ct * 64 + c;
  f32x4 y[4];
#pragma unroll
  for (int j = 0; j < 4; j++) y[j] = *(const f32x4*)(yp + 4 * j);
  const u32x4 xa = *(const u32x4*)(x + (size_t)(rt * 64 + r) * 512 + ct * 64 + c), xb = *(const u32x4*)(x + (size_t)(rt * 64 + r) * 512 + ct * 64 + c + 8);
  const u32x4 ba = *(const u32x4*)(b2 + ct * 64 + c), bb = *(const u32x4*)(b2 + ct * 64 + c + 8);
  VMCNT(0);
  const unsigned tr0 = ring + (4 * (lane >> 5) + ((lane & 15) >> 2)) * 128 + ((lane >> 4) & 1) * 32 + (lane & 3) * 8;
  f32x16 acc[4] = {};
#pragma unroll
  for (int s = 0; s < 8; s++) {
    const bf16x8 wa = lds_tr2(tr0 + s * 2048, 1024), wb = lds_tr2(tr0 + s * 2048 + 64, 1024);
    acc[0] = MFMA(wa, of[s][0], acc[0]); acc[1] = MFMA(wb, of[s][0], acc[1]); acc[2] = MFMA(wa, of[s][1], acc[2]); acc[3] = MFMA(wb, of[s][1], acc[3]);
  }
  const unsigned base = g * (64 * 272);
#pragma unroll
  for (int a = 0; a < 4; a++)
#pragma unroll
    for (int q = 0; q < 4; q++) {
      f32x4 v = {acc[a][4 * q], acc[a][4 * q + 1], acc[a][4 * q + 2], acc[a][4 * q + 3]};
      lds_wf4(base + ((a >> 1) * 32 + t) * 272 + ((a & 1) * 32 + 8 * q + 4 * h) * 4, v);
    }
  BAR();
#pragma unroll
  for (int q = 0; q < 4; q++)
#pragma unroll
    for (int j = 0; j < 4; j++) y[j] += lds_f4(q * (64 * 272) + r * 272 + c * 4 + j * 16);
  u32x4 o[2];
#pragma unroll
  for (int j = 0; j < 4; j++) {
    const unsigned x0 = j < 2 ? xa[2 * j] : xb[2 * j - 4], x1 = j < 2 ? xa[2 * j + 1] : xb[2 * j - 3];
    const unsigned c0 = j < 2 ? ba[2 * j] : bb[2 * j - 4], c1 = j < 2 ? ba[2 * j + 1] : bb[2 * j - 3];
    const float v0 = y[j][0] + __uint_as_float(x0 << 16) + __uint_as_float(c0 << 16), v1 = y[j][1] + __uint_as_float(x0 & 0xffff0000u) + __uint_as_float(c0 & 0xffff0000u);
    const float v2 = y[j][2] + __uint_as_float(x1 << 16) + __uint_as_float(c1 << 16), v3 = y[j][3] + __uint_as_float(x1 & 0xffff0000u) + __uint_as_float(c1 & 0xffff0000u);
    o[j >> 1][(j & 1) * 2] = pk2(v0, v1); o[j >> 1][(j & 1) * 2 + 1] = pk2(v2, v3);
  }
  u16* outp = out + (size_t)(rt * 64 + r) * 512 + ct * 64 + c;
  *(u32x4*)outp = o[0]; *(u32x4*)(outp + 8) = o[1];
}

extern "C" void kernel_launch(void* const* d_in, const int* in_sizes, int n_in, void* d_out, int out_size, void* d_ws, size_t ws_size,
                              hipStream_t stream) {
  const u16* x = (const u16*)d_in[0];
  (void)hipFuncSetAttribute((const void*)k_phase1, hipFuncAttributeMaxDynamicSharedMemorySize, LDS_BYTES);
  (void)hipFuncSetAttribute((const void*)k_phase2, hipFuncAttributeMaxDynamicSharedMemorySize, LDS_BYTES);
  (void)hipFuncSetAttribute((const void*)k_phase3, hipFuncAttributeMaxDynamicSharedMemorySize, 135168);
  k_phase1<<<256, 512, LDS_BYTES, stream>>>(x, (const u16*)d_in[1], (const u16*)d_in[4], (const u16*)d_in[5], (char*)d_ws);
  k_phase2<<<256, 512, LDS_BYTES, stream>>>((const u16*)d_in[6], (char*)d_ws);
  k_phase3<<<256, 256, 135168, stream>>>(x, (const u16*)d_in[2], (const u16*)d_in[7], (const char*)d_ws, (u16*)d_out);
}
```

```cpp
#include <hip/hip_runtime.h>
#include <hip/hip_bf16.h>
#include <stdint.h>

typedef __attribute__((ext_vector_type(8))) __bf16 bf16x8;
typedef __attribute__((ext_vector_type(4))) __bf16 bf16x4;
typedef __attribute__((ext_vector_type(2))) __bf16 bf16x2;
typedef __attribute__((ext_vector_type(2))) float f32x2;
typedef __attribute__((ext_vector_type(4))) float f32x4;
typedef __attribute__((ext_vector_type(16))) float f32x16;
typedef __attribute__((ext_vector_type(4))) unsigned u32x4;
typedef __attribute__((ext_vector_type(2))) unsigned u32x2;
typedef unsigned short u16;
typedef unsigned long long u64;
#define LDSP __attribute__((address_space(3)))
#define GLBP __attribute__((address_space(1)))
#define DI __device__ __forceinline__
#define MFMA(a, b, c) __builtin_amdgcn_mfma_f32_32x32x16_bf16((a), (b), (c), 0, 0, 0)

constexpr unsigned WS_EPOCH = 0, WS_FQ = 4096  , WS_FH = 8192  ;
constexpr unsigned WS_RS = 16384;
constexpr unsigned WS_Q = 1u << 20;
constexpr unsigned WS_KV = 4u << 20;
constexpr unsigned WS_H = 8u << 20;
constexpr unsigned WS_O = 16u << 20;
constexpr int LDS_BYTES = 163840;
DI void dma16(const void* sbase, unsigned voff, unsigned lds_dst) {
  unsigned keep;
  asm volatile("s_mov_b32 %0, m0\n\ts_mov_b32 m0, %3\n\ts_nop 0\n\tglobal_load_lds_dwordx4 %1, %2\n\ts_mov_b32 m0, %0"
               : "=&s"(keep) : "v"(voff), "s"(sbase), "s"(lds_dst) : "memory");
}
DI void dma16_sc(const void* sbase, unsigned voff, unsigned lds_dst) {
  unsigned keep;
  asm volatile("s_mov_b32 %0, m0\n\ts_mov_b32 m0, %3\n\ts_nop 0\n\tglobal_load_lds_dwordx4 %1, %2 sc1\n\ts_mov_b32 m0, %0"
               : "=&s"(keep) : "v"(voff), "s"(sbase), "s"(lds_dst) : "memory");
}
DI void gload_sc(u32x4& d, const void* sbase, unsigned voff) { asm volatile("global_load_dwordx4 %0, %1, %2 sc1" : "=v"(d) : "v"(voff), "s"(sbase) : "memory"); }
DI void gstore_sc(const void* sbase, unsigned voff, u32x4 v) { asm volatile("global_store_dwordx4 %0, %1, %2 sc1\n\ts_nop 1" :: "v"(voff), "v"(v), "s"(sbase) : "memory"); }
#define VMCNT(n) asm volatile("s_waitcnt vmcnt(" #n ")" ::: "memory")
#define LGKM0() asm volatile("s_waitcnt lgkmcnt(0)" ::: "memory")
#define BAR() asm volatile("s_waitcnt lgkmcnt(0)\n\ts_barrier" ::: "memory")
#define GAP(a) asm volatile("s_nop 15" : "+v"(a))
#define PIN4(a) asm volatile("" : "+v"((a)[0]), "+v"((a)[1]), "+v"((a)[2]), "+v"((a)[3]))

DI bf16x8 lds_b128(unsigned a) { return *(LDSP bf16x8*)(uintptr_t)a; }
DI f32x4 lds_f4(unsigned a) { return *(LDSP f32x4*)(uintptr_t)a; }
DI void lds_wf1(unsigned a, float v) { *(LDSP float*)(uintptr_t)a = v; }
DI void lds_wu4(unsigned a, u32x4 v) { *(LDSP u32x4*)(uintptr_t)a = v; }
DI void lds_wf4(unsigned a, f32x4 v) { *(LDSP f32x4*)(uintptr_t)a = v; }
DI bf16x8 lds_tr2(unsigned a, unsigned d) {
  bf16x4 lo = __builtin_amdgcn_ds_read_tr16_b64_v4bf16((LDSP bf16x4*)(uintptr_t)a);
  bf16x4 hi = __builtin_amdgcn_ds_read_tr16_b64_v4bf16((LDSP bf16x4*)(uintptr_t)(a + d));
  return __builtin_shufflevector(lo, hi, 0, 1, 2, 3, 4, 5, 6, 7);
}
DI unsigned pk2(float a, float b) { f32x2 v = {a, b}; bf16x2 c = __builtin_convertvector(v, bf16x2); return __builtin_bit_cast(unsigned, c); }
DI float rbf(float a) { return __uint_as_float(pk2(a, a) << 16); }
DI float half_sum(float v) {
  unsigned u = __float_as_uint(v);
  auto r = __builtin_amdgcn_permlane32_swap(u, u, false, false);
  unsigned r0 = r[0], r1 = r[1];
  return __uint_as_float(r0) + __uint_as_float(r1);
}
DI float half_max(float v) {
  unsigned u = __float_as_uint(v);
  auto r = __builtin_amdgcn_permlane32_swap(u, u, false, false);
  unsigned r0 = r[0], r1 = r[1];
  return fmaxf(__uint_as_float(r0), __uint_as_float(r1));
}
DI float half_pick(float a, float b) {
  auto r = __builtin_amdgcn_permlane32_swap(__float_as_uint(a), __float_as_uint(b), false, false);
  unsigned r0 = r[0], r1 = r[1];
  return __uint_as_float(r0) + __uint_as_float(r1);
}
template <int CTRL, int RM> DI float dpp(float v) { return __builtin_bit_cast(float, __builtin_amdgcn_update_dpp(0, __builtin_bit_cast(int, v), CTRL, RM, 0xf, false)); }
DI void wait_flags(const char* ws, unsigned off, u64 tag) {
#pragma nounroll
  for (unsigned spin = 0; spin < (1u << 18); spin++) {
    u64 v = tag;
    if (off != 0xffffffffu) v = __hip_atomic_load((GLBP u64*)(ws + off), __ATOMIC_RELAXED, __HIP_MEMORY_SCOPE_AGENT);
    if (__all(v == tag)) break;
    __builtin_amdgcn_s_sleep(2);
  }
  asm volatile("" ::: "memory");
}
DI void put_flag(char* ws, unsigned off, u64 tag) { __hip_atomic_store((GLBP u64*)(ws + off), tag, __ATOMIC_RELAXED, __HIP_MEMORY_SCOPE_AGENT); }
DI void lds_add(unsigned cnt) { asm volatile("s_mov_b64 exec, 1\n\tds_add_u32 %0, %1\n\ts_mov_b64 exec, -1" :: "v"(cnt), "v"(1u) : "memory"); }
DI void lds_wait(unsigned cnt, unsigned n) {
  unsigned v, s, k = 1u << 22;
  asm volatile("1:\n\tds_read_b32 %0, %3\n\ts_waitcnt lgkmcnt(0)\n\tv_readfirstlane_b32 %1, %0\n\ts_cmp_ge_u32 %1, %4\n\ts_cbranch_scc1 2f\n\t"
               "s_sleep 1\n\ts_sub_u32 %2, %2, 1\n\ts_cbranch_scc0 1b\n2:"
               : "=&v"(v), "=&s"(s), "+s"(k) : "v"(cnt), "i"(n) : "scc", "memory");
}
DI void arrive(unsigned cnt, const char* ws, unsigned off, u64 tag) {
  unsigned v, s;
  asm volatile("s_mov_b64 exec, 1\n\tds_add_rtn_u32 %0, %2, %3\n\ts_waitcnt lgkmcnt(0)\n\tv_readfirstlane_b32 %1, %0\n\ts_cmp_lg_u32 %1, 3\n\ts_cbranch_scc1 1f\n\t"
               "global_store_dwordx2 %4, %5, %6 sc1\n1:\n\ts_mov_b64 exec, -1"
               : "=&v"(v), "=&s"(s) : "v"(cnt), "v"(1u), "v"(off), "v"(tag), "s"(ws) : "scc", "memory");
}

constexpr unsigned L_X = 0, L_QW = 65536, L_FW1 = 0, L_FW2 = 65536, L_KV = 0;
constexpr unsigned SCR = 65536, SCR_CS = SCR  , SCR_RA = SCR + 2048  ;
constexpr unsigned MISC = 131072  , CNT = MISC + 64,
                   M_CP = MISC + 2688  ;
constexpr unsigned C_FW1 = CNT + 36, C_ST = CNT + 4, C_FW2 = CNT + 8, C_F1 = CNT + 12, C_KV1 = CNT + 16, C_CS = CNT + 20, C_KV2 = CNT + 24, C_H = CNT + 28, C_FQ = CNT + 32, C_FH = CNT;
constexpr unsigned RING = 139264;

#define XIN_LO "{v[128:143]}"(X[0]), "{v[144:159]}"(X[1]), "{v[160:175]}"(X[2]), "{v[176:191]}"(X[3])
#define XIN_HI "{v[192:207]}"(X[4]), "{v[208:223]}"(X[5]), "{v[224:239]}"(X[6]), "{v[240:255]}"(X[7])
#define XADR [a0] "v"(xa[0]), [a1] "v"(xa[1]), [a2] "v"(xa[2]), [a3] "v"(xa[3]), [a4] "v"(xa[4]), [a5] "v"(xa[5]), [a6] "v"(xa[6]), [a7] "v"(xa[7])
#define B4 [b0] "v"(wb[0]), [b1] "v"(wb[1]), [b2] "v"(wb[2]), [b3] "v"(wb[3])
#define G16 [g0] "v"(gb[0]), [g1] "v"(gb[1]), [g2] "v"(gb[2]), [g3] "v"(gb[3]), [g4] "v"(gb[4]), [g5] "v"(gb[5]), [g6] "v"(gb[6]), [g7] "v"(gb[7]), \
            [g8] "v"(gb[8]), [g9] "v"(gb[9]), [g10] "v"(gb[10]), [g11] "v"(gb[11]), [g12] "v"(gb[12]), [g13] "v"(gb[13]), [g14] "v"(gb[14]), [g15] "v"(gb[15])
#define GCLOB "v32", "v33", "v34", "v35", "v36", "v37", "v38", "v39", "v40", "v41", "v42", "v43", "v44", "v45", "v46", "v47", "v48", "v49", "v50", "v51", "v52", "v53", "v54", "v55", "v56", "v57", "v58", "v59", "v60", "v61", "v62", "v63"
#define QCLOB "v48", "v49", "v50", "v51", "v52", "v53", "v54", "v55", "v56", "v57", "v58", "v59", "v60", "v61", "v62", "v63", "v64", "v65", "v66", "v67", "v68", "v69", "v70", "v71", "v72", "v73", "v74", "v75", "v76", "v77", "v78", "v79"
#define CSUM [c0] "+v"(c0), [c1] "+v"(c1), [c2] "+v"(c2), [t0] "=&v"(t0), [t1] "=&v"(t1)

struct AttnState { f32x16 oa, ob; u32x4 p, v; };
DI void attn_init(AttnState& s) {
  asm volatile(
      "v_mov_b64 v[208:209], 0\n"
      "v_mov_b64 v[210:211], 0\n"
      "v_mov_b64 v[212:213], 0\n"
      "v_mov_b64 v[214:215], 0\n"
      "v_mov_b64 v[216:217], 0\n"
      "v_mov_b64 v[218:219], 0\n"
      "v_mov_b64 v[220:221], 0\n"
      "v_mov_b64 v[222:223], 0\n"
      "v_mov_b64 v[224:225], 0\n"
      "v_mov_b64 v[226:227], 0\n"
      "v_mov_b64 v[228:229], 0\n"
      "v_mov_b64 v[230:231], 0\n"
      "v_mov_b64 v[232:233], 0\n"
      "v_mov_b64 v[234:235], 0\n"
      "v_mov_b64 v[236:237], 0\n"
      "v_mov_b64 v[238:239], 0\n"
      "v_mov_b64 v[204:205], 0\n"
      "v_mov_b64 v[206:207], 0\n"
      "v_mov_b64 v[116:117], 0\n"
      "v_mov_b64 v[118:119], 0\n"
      : "={v[208:223]}"(s.oa), "={v[224:239]}"(s.ob), "={v[204:207]}"(s.p), "={v[116:119]}"(s.v));
}
DI void attn_loop(u32x4 qa, u32x4 qb, f32x16& na, f32x16& nb, int given, unsigned ka, unsigned va, unsigned vs, AttnState& s) {
  asm volatile(
      "ds_read_b128 v[240:243], v252\n"
      "ds_read_b128 v[108:111], v252 offset:2048\n"
      "ds_read_b128 v[244:247], v253\n"
      "ds_read_b128 v[248:251], v253 offset:512\n"
      "s_mov_b32 s40, 8\n"
      "s_cmp_lg_u32 %[given], 0\n"
      "s_waitcnt lgkmcnt(3)\n"
      "s_cbranch_scc1 9f\n"
      "v_mfma_f32_32x32x16_bf16 v[168:183], v[240:243], v[128:131], 0\n"
      "v_mfma_f32_32x32x16_bf16 v[184:199], v[240:243], v[132:135], 0\n"
      "s_nop 10\n"
      "v_max3_f32 v136, v168, v169, v170\n"
      "v_max3_f32 v137, v171, v172, v173\n"
      "v_max3_f32 v136, v174, v175, v136\n"
      "v_max3_f32 v137, v176, v177, v137\n"
      "v_max3_f32 v136, v178, v179, v136\n"
      "v_max3_f32 v137, v180, v181, v137\n"
      "v_max3_f32 v136, v182, v183, v136\n"
      "v_max_f32 v136, v136, v137\n"
      "s_nop 1\n"
      "v_max3_f32 v152, v184, v185, v186\n"
      "v_max3_f32 v153, v187, v188, v189\n"
      "v_max3_f32 v152, v190, v191, v152\n"
      "v_max3_f32 v153, v192, v193, v153\n"
      "v_max3_f32 v152, v194, v195, v152\n"
      "v_max3_f32 v153, v196, v197, v153\n"
      "v_max3_f32 v152, v198, v199, v152\n"
      "v_max_f32 v152, v152, v153\n"
      "v_mov_b32 v137, v136\n"
      "v_mov_b32 v153, v152\n"
      "s_nop 1\n"
      "v_permlane32_swap_b32 v136, v137\n"
      "v_permlane32_swap_b32 v152, v153\n"
      "s_nop 1\n"
      "v_max_f32 v136, v136, v137\n"
      "v_max_f32 v152, v152, v153\n"
      "v_xor_b32 v136, 0x80000000, v136\n"
      "v_xor_b32 v152, 0x80000000, v152\n"
      "v_mov_b32 v137, v136\n"
      "v_mov_b32 v153, v152\n"
      "v_mov_b64 v[138:139], v[136:137]\n"
      "v_mov_b64 v[140:141], v[136:137]\n"
      "v_mov_b64 v[142:143], v[136:137]\n"
      "v_mov_b64 v[144:145], v[136:137]\n"
      "v_mov_b64 v[146:147], v[136:137]\n"
      "v_mov_b64 v[148:149], v[136:137]\n"
      "v_mov_b64 v[150:151], v[136:137]\n"
      "v_mov_b64 v[154:155], v[152:153]\n"
      "v_mov_b64 v[156:157], v[152:153]\n"
      "v_mov_b64 v[158:159], v[152:153]\n"
      "v_mov_b64 v[160:161], v[152:153]\n"
      "v_mov_b64 v[162:163], v[152:153]\n"
      "v_mov_b64 v[164:165], v[152:153]\n"
      "v_mov_b64 v[166:167], v[152:153]\n"
      "9:\n"
      "v_mfma_f32_32x32x16_bf16 v[168:183], v[240:243], v[128:131], v[136:151]\n"
      "v_mov_b64 v[198:199], 0\n"
      "s_waitcnt lgkmcnt(0)\n"
      "s_nop 9\n"
      "1:\n"
      "v_exp_f32 v168, v168\n"
      "v_cvt_pk_bf16_f32 v207, v198, v199\n"
      "v_exp_f32 v169, v169\n"
      "v_exp_f32 v170, v170\n"
      "v_mfma_f32_32x32x16_bf16 v[224:239], v[116:119], v[204:207], v[224:239]\n"
      "ds_read_b128 v[116:119], v253 offset:2560\n"
      "v_cvt_pk_bf16_f32 v200, v168, v169\n"
      "v_exp_f32 v171, v171\n"
      "v_exp_f32 v172, v172\n"
      "v_cvt_pk_bf16_f32 v201, v170, v171\n"
      "v_exp_f32 v173, v173\n"
      "v_exp_f32 v174, v174\n"
      "v_cvt_pk_bf16_f32 v202, v172, v173\n"
      "v_mfma_f32_32x32x16_bf16 v[184:199], v[240:243], v[132:135], v[152:167]\n"
      "ds_read_b128 v[240:243], v252 offset:4096\n"
      "v_exp_f32 v175, v175\n"
      "v_exp_f32 v176, v176\n"
      "v_cvt_pk_bf16_f32 v203, v174, v175\n"
      "v_exp_f32 v177, v177\n"
      "v_exp_f32 v178, v178\n"
      "v_cvt_pk_bf16_f32 v204, v176, v177\n"
      "v_exp_f32 v179, v179\n"
      "v_mfma_f32_32x32x16_bf16 v[208:223], v[244:247], v[200:203], v[208:223]\n"
      "ds_read_b128 v[112:115], v253 offset:2048\n"
      "v_exp_f32 v180, v180\n"
      "v_cvt_pk_bf16_f32 v205, v178, v179\n"
      "v_exp_f32 v181, v181\n"
      "v_exp_f32 v182, v182\n"
      "v_cvt_pk_bf16_f32 v206, v180, v181\n"
      "v_exp_f32 v183, v183\n"
      "v_exp_f32 v184, v184\n"
      "v_cvt_pk_bf16_f32 v207, v182, v183\n"
      "v_exp_f32 v185, v185\n"
      "v_exp_f32 v186, v186\n"
      "v_mfma_f32_32x32x16_bf16 v[208:223], v[248:251], v[204:207], v[208:223]\n"
      "v_cvt_pk_bf16_f32 v200, v184, v185\n"
      "v_exp_f32 v187, v187\n"
      "v_exp_f32 v188, v188\n"
      "v_cvt_pk_bf16_f32 v201, v186, v187\n"
      "v_exp_f32 v189, v189\n"
      "v_exp_f32 v190, v190\n"
      "v_cvt_pk_bf16_f32 v202, v188, v189\n"
      "v_mfma_f32_32x32x16_bf16 v[168:183], v[108:111], v[128:131], v[136:151]\n"
      "v_exp_f32 v191, v191\n"
      "v_exp_f32 v192, v192\n"
      "v_cvt_pk_bf16_f32 v203, v190, v191\n"
      "v_exp_f32 v193, v193\n"
      "v_exp_f32 v194, v194\n"
      "v_cvt_pk_bf16_f32 v204, v192, v193\n"
      "v_exp_f32 v195, v195\n"
      "v_mfma_f32_32x32x16_bf16 v[224:239], v[244:247], v[200:203], v[224:239]\n"
      "s_waitcnt lgkmcnt(0)\n"
      "v_exp_f32 v196, v196\n"
      "v_cvt_pk_bf16_f32 v205, v194, v195\n"
      "v_exp_f32 v197, v197\n"
      "v_exp_f32 v198, v198\n"
      "v_cvt_pk_bf16_f32 v206, v196, v197\n"
      "v_exp_f32 v199, v199\n"
      "v_exp_f32 v168, v168\n"
      "v_cvt_pk_bf16_f32 v207, v198, v199\n"
      "v_exp_f32 v169, v169\n"
      "v_exp_f32 v170, v170\n"
      "v_mfma_f32_32x32x16_bf16 v[224:239], v[248:251], v[204:207], v[224:239]\n"
      "ds_read_b128 v[248:251], v253 offset:4608\n"
      "v_cvt_pk_bf16_f32 v200, v168, v169\n"
      "v_exp_f32 v171, v171\n"
      "v_exp_f32 v172, v172\n"
      "v_cvt_pk_bf16_f32 v201, v170, v171\n"
      "v_exp_f32 v173, v173\n"
      "v_exp_f32 v174, v174\n"
      "v_cvt_pk_bf16_f32 v202, v172, v173\n"
      "v_mfma_f32_32x32x16_bf16 v[184:199], v[108:111], v[132:135], v[152:167]\n"
      "ds_read_b128 v[108:111], v252 offset:6144\n"
      "v_exp_f32 v175, v175\n"
      "v_exp_f32 v176, v176\n"
      "v_cvt_pk_bf16_f32 v203, v174, v175\n"
      "v_exp_f32 v177, v177\n"
      "v_exp_f32 v178, v178\n"
      "v_cvt_pk_bf16_f32 v204, v176, v177\n"
      "v_exp_f32 v179, v179\n"
      "v_mfma_f32_32x32x16_bf16 v[208:223], v[112:115], v[200:203], v[208:223]\n"
      "ds_read_b128 v[244:247], v253 offset:4096\n"
      "v_exp_f32 v180, v180\n"
      "v_cvt_pk_bf16_f32 v205, v178, v179\n"
      "v_exp_f32 v181, v181\n"
      "v_exp_f32 v182, v182\n"
      "v_cvt_pk_bf16_f32 v206, v180, v181\n"
      "v_exp_f32 v183, v183\n"
      "v_exp_f32 v184, v184\n"
      "v_cvt_pk_bf16_f32 v207, v182, v183\n"
      "v_exp_f32 v185, v185\n"
      "v_exp_f32 v186, v186\n"
      "v_mfma_f32_32x32x16_bf16 v[208:223], v[116:119], v[204:207], v[208:223]\n"
      "v_cvt_pk_bf16_f32 v200, v184, v185\n"
      "v_exp_f32 v187, v187\n"
      "v_exp_f32 v188, v188\n"
      "v_cvt_pk_bf16_f32 v201, v186, v187\n"
      "v_exp_f32 v189, v189\n"
      "v_exp_f32 v190, v190\n"
      "v_cvt_pk_bf16_f32 v202, v188, v189\n"
      "v_mfma_f32_32x32x16_bf16 v[168:183], v[240:243], v[128:131], v[136:151]\n"
      "v_exp_f32 v191, v191\n"
      "v_exp_f32 v192, v192\n"
      "v_cvt_pk_bf16_f32 v203, v190, v191\n"
      "v_exp_f32 v193, v193\n"
      "v_exp_f32 v194, v194\n"
      "v_cvt_pk_bf16_f32 v204, v192, v193\n"
      "v_exp_f32 v195, v195\n"
      "v_mfma_f32_32x32x16_bf16 v[224:239], v[112:115], v[200:203], v[224:239]\n"
      "s_waitcnt lgkmcnt(0)\n"
      "v_exp_f32 v196, v196\n"
      "v_cvt_pk_bf16_f32 v205, v194, v195\n"
      "v_exp_f32 v197, v197\n"
      "v_exp_f32 v198, v198\n"
      "v_cvt_pk_bf16_f32 v206, v196, v197\n"
      "v_exp_f32 v199, v199\n"
      "v_add_u32 v252, 0x2000, v252\n"
      "v_exp_f32 v168, v168\n"
      "v_cvt_pk_bf16_f32 v207, v198, v199\n"
      "v_exp_f32 v169, v169\n"
      "v_exp_f32 v170, v170\n"
      "v_mfma_f32_32x32x16_bf16 v[224:239], v[116:119], v[204:207], v[224:239]\n"
      "ds_read_b128 v[116:119], v253 offset:6656\n"
      "v_cvt_pk_bf16_f32 v200, v168, v169\n"
      "v_exp_f32 v171, v171\n"
      "v_exp_f32 v172, v172\n"
      "v_cvt_pk_bf16_f32 v201, v170, v171\n"
      "v_exp_f32 v173, v173\n"
      "v_exp_f32 v174, v174\n"
      "v_cvt_pk_bf16_f32 v202, v172, v173\n"
      "v_mfma_f32_32x32x16_bf16 v[184:199], v[240:243], v[132:135], v[152:167]\n"
      "ds_read_b128 v[240:243], v252 offset:0\n"
      "v_exp_f32 v175, v175\n"
      "v_exp_f32 v176, v176\n"
      "v_cvt_pk_bf16_f32 v203, v174, v175\n"
      "v_exp_f32 v177, v177\n"
      "v_exp_f32 v178, v178\n"
      "v_cvt_pk_bf16_f32 v204, v176, v177\n"
      "v_exp_f32 v179, v179\n"
      "v_mfma_f32_32x32x16_bf16 v[208:223], v[244:247], v[200:203], v[208:223]\n"
      "ds_read_b128 v[112:115], v253 offset:6144\n"
      "v_exp_f32 v180, v180\n"
      "v_cvt_pk_bf16_f32 v205, v178, v179\n"
      "v_exp_f32 v181, v181\n"
      "v_exp_f32 v182, v182\n"
      "v_cvt_pk_bf16_f32 v206, v180, v181\n"
      "v_exp_f32 v183, v183\n"
      "v_exp_f32 v184, v184\n"
      "v_cvt_pk_bf16_f32 v207, v182, v183\n"
      "v_exp_f32 v185, v185\n"
      "v_exp_f32 v186, v186\n"
      "v_mfma_f32_32x32x16_bf16 v[208:223], v[248:251], v[204:207], v[208:223]\n"
      "v_cvt_pk_bf16_f32 v200, v184, v185\n"
      "v_exp_f32 v187, v187\n"
      "v_exp_f32 v188, v188\n"
      "v_cvt_pk_bf16_f32 v201, v186, v187\n"
      "v_exp_f32 v189, v189\n"
      "v_exp_f32 v190, v190\n"
      "v_cvt_pk_bf16_f32 v202, v188, v189\n"
      "v_mfma_f32_32x32x16_bf16 v[168:183], v[108:111], v[128:131], v[136:151]\n"
      "v_exp_f32 v191, v191\n"
      "v_exp_f32 v192, v192\n"
      "v_cvt_pk_bf16_f32 v203, v190, v191\n"
      "v_exp_f32 v193, v193\n"
      "v_exp_f32 v194, v194\n"
      "v_cvt_pk_bf16_f32 v204, v192, v193\n"
      "v_exp_f32 v195, v195\n"
      "v_mfma_f32_32x32x16_bf16 v[224:239], v[244:247], v[200:203], v[224:239]\n"
      "s_waitcnt lgkmcnt(0)\n"
      "v_exp_f32 v196, v196\n"
      "v_cvt_pk_bf16_f32 v205, v194, v195\n"
      "v_exp_f32 v197, v197\n"
      "v_exp_f32 v198, v198\n"
      "v_cvt_pk_bf16_f32 v206, v196, v197\n"
      "v_exp_f32 v199, v199\n"
      "v_add_u32 v253, v254, v253\n"
      "v_exp_f32 v168, v168\n"
      "v_cvt_pk_bf16_f32 v207, v198, v199\n"
      "v_exp_f32 v169, v169\n"
      "v_exp_f32 v170, v170\n"
      "v_mfma_f32_32x32x16_bf16 v[224:239], v[248:251], v[204:207], v[224:239]\n"
      "ds_read_b128 v[248:251], v253 offset:512\n"
      "v_cvt_pk_bf16_f32 v200, v168, v169\n"
      "v_exp_f32 v171, v171\n"
      "v_exp_f32 v172, v172\n"
      "v_cvt_pk_bf16_f32 v201, v170, v171\n"
      "v_exp_f32 v173, v173\n"
      "v_exp_f32 v174, v174\n"
      "v_cvt_pk_bf16_f32 v202, v172, v173\n"
      "v_mfma_f32_32x32x16_bf16 v[184:199], v[108:111], v[132:135], v[152:167]\n"
      "ds_read_b128 v[108:111], v252 offset:2048\n"
      "v_exp_f32 v175, v175\n"
      "v_exp_f32 v176, v176\n"
      "v_cvt_pk_bf16_f32 v203, v174, v175\n"
      "v_exp_f32 v177, v177\n"
      "v_exp_f32 v178, v178\n"
      "v_cvt_pk_bf16_f32 v204, v176, v177\n"
      "v_exp_f32 v179, v179\n"
      "v_mfma_f32_32x32x16_bf16 v[208:223], v[112:115], v[200:203], v[208:223]\n"
      "ds_read_b128 v[244:247], v253 offset:0\n"
      "v_exp_f32 v180, v180\n"
      "v_cvt_pk_bf16_f32 v205, v178, v179\n"
      "v_exp_f32 v181, v181\n"
      "v_exp_f32 v182, v182\n"
      "v_cvt_pk_bf16_f32 v206, v180, v181\n"
      "v_exp_f32 v183, v183\n"
      "v_exp_f32 v184, v184\n"
      "v_cvt_pk_bf16_f32 v207, v182, v183\n"
      "v_exp_f32 v185, v185\n"
      "v_exp_f32 v186, v186\n"
      "v_mfma_f32_32x32x16_bf16 v[208:223], v[116:119], v[204:207], v[208:223]\n"
      "v_cvt_pk_bf16_f32 v200, v184, v185\n"
      "v_exp_f32 v187, v187\n"
      "v_exp_f32 v188, v188\n"
      "v_cvt_pk_bf16_f32 v201, v186, v187\n"
      "v_exp_f32 v189, v189\n"
      "v_exp_f32 v190, v190\n"
      "v_cvt_pk_bf16_f32 v202, v188, v189\n"
      "v_mfma_f32_32x32x16_bf16 v[168:183], v[240:243], v[128:131], v[136:151]\n"
      "v_exp_f32 v191, v191\n"
      "v_exp_f32 v192, v192\n"
      "v_cvt_pk_bf16_f32 v203, v190, v191\n"
      "v_exp_f32 v193, v193\n"
      "v_exp_f32 v194, v194\n"
      "v_cvt_pk_bf16_f32 v204, v192, v193\n"
      "v_exp_f32 v195, v195\n"
      "v_mfma_f32_32x32x16_bf16 v[224:239], v[112:115], v[200:203], v[224:239]\n"
      "s_waitcnt lgkmcnt(0)\n"
      "v_exp_f32 v196, v196\n"
      "v_cvt_pk_bf16_f32 v205, v194, v195\n"
      "v_exp_f32 v197, v197\n"
      "v_exp_f32 v198, v198\n"
      "v_cvt_pk_bf16_f32 v206, v196, v197\n"
      "v_exp_f32 v199, v199\n"
      "v_exp_f32 v168, v168\n"
      "v_cvt_pk_bf16_f32 v207, v198, v199\n"
      "v_exp_f32 v169, v169\n"
      "v_exp_f32 v170, v170\n"
      "v_mfma_f32_32x32x16_bf16 v[224:239], v[116:119], v[204:207], v[224:239]\n"
      "ds_read_b128 v[116:119], v253 offset:2560\n"
      "v_cvt_pk_bf16_f32 v200, v168, v169\n"
      "v_exp_f32 v171, v171\n"
      "v_exp_f32 v172, v172\n"
      "v_cvt_pk_bf16_f32 v201, v170, v171\n"
      "v_exp_f32 v173, v173\n"
      "v_exp_f32 v174, v174\n"
      "v_cvt_pk_bf16_f32 v202, v172, v173\n"
      "v_mfma_f32_32x32x16_bf16 v[184:199], v[240:243], v[132:135], v[152:167]\n"
      "ds_read_b128 v[240:243], v252 offset:4096\n"
      "v_exp_f32 v175, v175\n"
      "v_exp_f32 v176, v176\n"
      "v_cvt_pk_bf16_f32 v203, v174, v175\n"
      "v_exp_f32 v177, v177\n"
      "v_exp_f32 v178, v178\n"
      "v_cvt_pk_bf16_f32 v204, v176, v177\n"
      "v_exp_f32 v179, v179\n"
      "v_mfma_f32_32x32x16_bf16 v[208:223], v[244:247], v[200:203], v[208:223]\n"
      "ds_read_b128 v[112:115], v253 offset:2048\n"
      "v_exp_f32 v180, v180\n"
      "v_cvt_pk_bf16_f32 v205, v178, v179\n"
      "v_exp_f32 v181, v181\n"
      "v_exp_f32 v182, v182\n"
      "v_cvt_pk_bf16_f32 v206, v180, v181\n"
      "v_exp_f32 v183, v183\n"
      "v_exp_f32 v184, v184\n"
      "v_cvt_pk_bf16_f32 v207, v182, v183\n"
      "v_exp_f32 v185, v185\n"
      "v_exp_f32 v186, v186\n"
      "v_mfma_f32_32x32x16_bf16 v[208:223], v[248:251], v[204:207], v[208:223]\n"
      "s_cmp_eq_u32 s40, 5\n"
      "v_cvt_pk_bf16_f32 v200, v184, v185\n"
      "v_exp_f32 v187, v187\n"
      "v_exp_f32 v188, v188\n"
      "v_cvt_pk_bf16_f32 v201, v186, v187\n"
      "v_exp_f32 v189, v189\n"
      "v_exp_f32 v190, v190\n"
      "v_cvt_pk_bf16_f32 v202, v188, v189\n"
      "v_mfma_f32_32x32x16_bf16 v[168:183], v[108:111], v[128:131], v[136:151]\n"
      "s_cbranch_scc1 5f\n"
      "6:\n"
      "v_exp_f32 v191, v191\n"
      "v_exp_f32 v192, v192\n"
      "v_cvt_pk_bf16_f32 v203, v190, v191\n"
      "v_exp_f32 v193, v193\n"
      "v_exp_f32 v194, v194\n"
      "v_cvt_pk_bf16_f32 v204, v192, v193\n"
      "v_exp_f32 v195, v195\n"
      "v_mfma_f32_32x32x16_bf16 v[224:239], v[244:247], v[200:203], v[224:239]\n"
      "s_waitcnt lgkmcnt(0)\n"
      "v_exp_f32 v196, v196\n"
      "v_cvt_pk_bf16_f32 v205, v194, v195\n"
      "v_exp_f32 v197, v197\n"
      "v_exp_f32 v198, v198\n"
      "v_cvt_pk_bf16_f32 v206, v196, v197\n"
      "v_exp_f32 v199, v199\n"
      "v_exp_f32 v168, v168\n"
      "v_cvt_pk_bf16_f32 v207, v198, v199\n"
      "v_exp_f32 v169, v169\n"
      "v_exp_f32 v170, v170\n"
      "v_mfma_f32_32x32x16_bf16 v[224:239], v[248:251], v[204:207], v[224:239]\n"
      "ds_read_b128 v[248:251], v253 offset:4608\n"
      "v_cvt_pk_bf16_f32 v200, v168, v169\n"
      "v_exp_f32 v171, v171\n"
      "v_exp_f32 v172, v172\n"
      "v_cvt_pk_bf16_f32 v201, v170, v171\n"
      "v_exp_f32 v173, v173\n"
      "v_exp_f32 v174, v174\n"
      "v_cvt_pk_bf16_f32 v202, v172, v173\n"
      "v_mfma_f32_32x32x16_bf16 v[184:199], v[108:111], v[132:135], v[152:167]\n"
      "ds_read_b128 v[108:111], v252 offset:6144\n"
      "v_exp_f32 v175, v175\n"
      "v_exp_f32 v176, v176\n"
      "v_cvt_pk_bf16_f32 v203, v174, v175\n"
      "v_exp_f32 v177, v177\n"
      "v_exp_f32 v178, v178\n"
      "v_cvt_pk_bf16_f32 v204, v176, v177\n"
      "v_exp_f32 v179, v179\n"
      "v_mfma_f32_32x32x16_bf16 v[208:223], v[112:115], v[200:203], v[208:223]\n"
      "ds_read_b128 v[244:247], v253 offset:4096\n"
      "v_exp_f32 v180, v180\n"
      "v_cvt_pk_bf16_f32 v205, v178, v179\n"
      "v_exp_f32 v181, v181\n"
      "v_exp_f32 v182, v182\n"
      "v_cvt_pk_bf16_f32 v206, v180, v181\n"
      "v_exp_f32 v183, v183\n"
      "v_exp_f32 v184, v184\n"
      "v_cvt_pk_bf16_f32 v207, v182, v183\n"
      "v_exp_f32 v185, v185\n"
      "v_exp_f32 v186, v186\n"
      "v_mfma_f32_32x32x16_bf16 v[208:223], v[116:119], v[204:207], v[208:223]\n"
      "v_cvt_pk_bf16_f32 v200, v184, v185\n"
      "v_exp_f32 v187, v187\n"
      "v_exp_f32 v188, v188\n"
      "v_cvt_pk_bf16_f32 v201, v186, v187\n"
      "v_exp_f32 v189, v189\n"
      "v_exp_f32 v190, v190\n"
      "v_cvt_pk_bf16_f32 v202, v188, v189\n"
      "v_mfma_f32_32x32x16_bf16 v[168:183], v[240:243], v[128:131], v[136:151]\n"
      "v_exp_f32 v191, v191\n"
      "v_exp_f32 v192, v192\n"
      "v_cvt_pk_bf16_f32 v203, v190, v191\n"
      "v_exp_f32 v193, v193\n"
      "v_exp_f32 v194, v194\n"
      "v_cvt_pk_bf16_f32 v204, v192, v193\n"
      "v_exp_f32 v195, v195\n"
      "v_mfma_f32_32x32x16_bf16 v[224:239], v[112:115], v[200:203], v[224:239]\n"
      "s_waitcnt lgkmcnt(0)\n"
      "v_exp_f32 v196, v196\n"
      "v_cvt_pk_bf16_f32 v205, v194, v195\n"
      "v_exp_f32 v197, v197\n"
      "v_exp_f32 v198, v198\n"
      "v_cvt_pk_bf16_f32 v206, v196, v197\n"
      "v_exp_f32 v199, v199\n"
      "v_add_u32 v252, 0x2000, v252\n"
      "v_exp_f32 v168, v168\n"
      "v_cvt_pk_bf16_f32 v207, v198, v199\n"
      "v_exp_f32 v169, v169\n"
      "v_exp_f32 v170, v170\n"
      "v_mfma_f32_32x32x16_bf16 v[224:239], v[116:119], v[204:207], v[224:239]\n"
      "ds_read_b128 v[116:119], v253 offset:6656\n"
      "v_cvt_pk_bf16_f32 v200, v168, v169\n"
      "v_exp_f32 v171, v171\n"
      "v_exp_f32 v172, v172\n"
      "v_cvt_pk_bf16_f32 v201, v170, v171\n"
      "v_exp_f32 v173, v173\n"
      "v_exp_f32 v174, v174\n"
      "v_cvt_pk_bf16_f32 v202, v172, v173\n"
      "v_mfma_f32_32x32x16_bf16 v[184:199], v[240:243], v[132:135], v[152:167]\n"
      "ds_read_b128 v[240:243], v252 offset:0\n"
      "v_exp_f32 v175, v175\n"
      "v_exp_f32 v176, v176\n"
      "v_cvt_pk_bf16_f32 v203, v174, v175\n"
      "v_exp_f32 v177, v177\n"
      "v_exp_f32 v178, v178\n"
      "v_cvt_pk_bf16_f32 v204, v176, v177\n"
      "v_exp_f32 v179, v179\n"
      "v_mfma_f32_32x32x16_bf16 v[208:223], v[244:247], v[200:203], v[208:223]\n"
      "ds_read_b128 v[112:115], v253 offset:6144\n"
      "v_exp_f32 v180, v180\n"
      "v_cvt_pk_bf16_f32 v205, v178, v179\n"
      "v_exp_f32 v181, v181\n"
      "v_exp_f32 v182, v182\n"
      "v_cvt_pk_bf16_f32 v206, v180, v181\n"
      "v_exp_f32 v183, v183\n"
      "v_exp_f32 v184, v184\n"
      "v_cvt_pk_bf16_f32 v207, v182, v183\n"
      "v_exp_f32 v185, v185\n"
      "v_exp_f32 v186, v186\n"
      "v_mfma_f32_32x32x16_bf16 v[208:223], v[248:251], v[204:207], v[208:223]\n"
      "v_cvt_pk_bf16_f32 v200, v184, v185\n"
      "v_exp_f32 v187, v187\n"
      "v_exp_f32 v188, v188\n"
      "v_cvt_pk_bf16_f32 v201, v186, v187\n"
      "v_exp_f32 v189, v189\n"
      "v_exp_f32 v190, v190\n"
      "v_cvt_pk_bf16_f32 v202, v188, v189\n"
      "v_mfma_f32_32x32x16_bf16 v[168:183], v[108:111], v[128:131], v[136:151]\n"
      "v_exp_f32 v191, v191\n"
      "v_exp_f32 v192, v192\n"
      "v_cvt_pk_bf16_f32 v203, v190, v191\n"
      "v_exp_f32 v193, v193\n"
      "v_exp_f32 v194, v194\n"
      "v_cvt_pk_bf16_f32 v204, v192, v193\n"
      "v_exp_f32 v195, v195\n"
      "v_mfma_f32_32x32x16_bf16 v[224:239], v[244:247], v[200:203], v[224:239]\n"
      "s_waitcnt lgkmcnt(0)\n"
      "v_exp_f32 v196, v196\n"
      "v_cvt_pk_bf16_f32 v205, v194, v195\n"
      "v_exp_f32 v197, v197\n"
      "v_exp_f32 v198, v198\n"
      "v_cvt_pk_bf16_f32 v206, v196, v197\n"
      "v_exp_f32 v199, v199\n"
      "v_add_u32 v253, v254, v253\n"
      "v_exp_f32 v168, v168\n"
      "v_cvt_pk_bf16_f32 v207, v198, v199\n"
      "v_exp_f32 v169, v169\n"
      "v_exp_f32 v170, v170\n"
      "v_mfma_f32_32x32x16_bf16 v[224:239], v[248:251], v[204:207], v[224:239]\n"
      "ds_read_b128 v[248:251], v253 offset:512\n"
      "v_cvt_pk_bf16_f32 v200, v168, v169\n"
      "v_exp_f32 v171, v171\n"
      "v_exp_f32 v172, v172\n"
      "v_cvt_pk_bf16_f32 v201, v170, v171\n"
      "v_exp_f32 v173, v173\n"
      "v_exp_f32 v174, v174\n"
      "v_cvt_pk_bf16_f32 v202, v172, v173\n"
      "v_mfma_f32_32x32x16_bf16 v[184:199], v[108:111], v[132:135], v[152:167]\n"
      "ds_read_b128 v[108:111], v252 offset:2048\n"
      "v_exp_f32 v175, v175\n"
      "v_exp_f32 v176, v176\n"
      "v_cvt_pk_bf16_f32 v203, v174, v175\n"
      "v_exp_f32 v177, v177\n"
      "v_exp_f32 v178, v178\n"
      "v_cvt_pk_bf16_f32 v204, v176, v177\n"
      "v_exp_f32 v179, v179\n"
      "v_mfma_f32_32x32x16_bf16 v[208:223], v[112:115], v[200:203], v[208:223]\n"
      "ds_read_b128 v[244:247], v253 offset:0\n"
      "v_exp_f32 v180, v180\n"
      "v_cvt_pk_bf16_f32 v205, v178, v179\n"
      "v_exp_f32 v181, v181\n"
      "v_exp_f32 v182, v182\n"
      "v_cvt_pk_bf16_f32 v206, v180, v181\n"
      "v_exp_f32 v183, v183\n"
      "v_exp_f32 v184, v184\n"
      "v_cvt_pk_bf16_f32 v207, v182, v183\n"
      "v_exp_f32 v185, v185\n"
      "v_exp_f32 v186, v186\n"
      "v_mfma_f32_32x32x16_bf16 v[208:223], v[116:119], v[204:207], v[208:223]\n"
      "s_sub_u32 s40, s40, 1\n"
      "v_cvt_pk_bf16_f32 v200, v184, v185\n"
      "v_exp_f32 v187, v187\n"
      "v_exp_f32 v188, v188\n"
      "v_cvt_pk_bf16_f32 v201, v186, v187\n"
      "v_exp_f32 v189, v189\n"
      "v_exp_f32 v190, v190\n"
      "v_cvt_pk_bf16_f32 v202, v188, v189\n"
      "v_mfma_f32_32x32x16_bf16 v[168:183], v[240:243], v[128:131], v[136:151]\n"
      "s_cmp_lg_u32 s40, 0\n"
      "v_exp_f32 v191, v191\n"
      "v_exp_f32 v192, v192\n"
      "v_cvt_pk_bf16_f32 v203, v190, v191\n"
      "v_exp_f32 v193, v193\n"
      "v_exp_f32 v194, v194\n"
      "v_cvt_pk_bf16_f32 v204, v192, v193\n"
      "v_exp_f32 v195, v195\n"
      "v_mfma_f32_32x32x16_bf16 v[224:239], v[112:115], v[200:203], v[224:239]\n"
      "s_waitcnt lgkmcnt(0)\n"
      "v_exp_f32 v196, v196\n"
      "v_cvt_pk_bf16_f32 v205, v194, v195\n"
      "v_exp_f32 v197, v197\n"
      "v_exp_f32 v198, v198\n"
      "v_cvt_pk_bf16_f32 v206, v196, v197\n"
      "v_exp_f32 v199, v199\n"
      "s_cbranch_scc1 1b\n"
      "s_branch 7f\n"
      "5:\n"
      "s_mov_b32 s41, 0x40000\n"
      "v_mov_b32 v120, %[kvc]\n"
      "8:\n"
      "ds_read_b32 v121, v120\n"
      "s_waitcnt lgkmcnt(0)\n"
      "v_readfirstlane_b32 s42, v121\n"
      "s_sub_u32 s41, s41, 1\n"
      "s_cbranch_scc1 6b\n"
      "s_cmp_lt_u32 s42, 4\n"
      "s_cbranch_scc1 8b\n"
      "s_branch 6b\n"
      "7:\n"
      "v_cvt_pk_bf16_f32 v207, v198, v199\n"
      "s_nop 1\n"
      "v_mfma_f32_32x32x16_bf16 v[224:239], v[116:119], v[204:207], v[224:239]\n"
      "s_nop 15\n"
      : "+{v[208:223]}"(s.oa), "+{v[224:239]}"(s.ob), "+{v[204:207]}"(s.p), "+{v[116:119]}"(s.v), "+{v[136:151]}"(na), "+{v[152:167]}"(nb), "+{v252}"(ka), "+{v253}"(va)
      : "{v[128:131]}"(qa), "{v[132:135]}"(qb), "{v254}"(vs), [kvc] "i"(C_KV2), [given] "s"(given)
      : "v108", "v109", "v110", "v111", "v112", "v113", "v114", "v115", "v120", "v121", "v168", "v169", "v170", "v171", "v172", "v173", "v174", "v175", "v176", "v177", "v178", "v179", "v180", "v181", "v182", "v183", "v184", "v185", "v186", "v187", "v188", "v189", "v190", "v191", "v192", "v193", "v194", "v195", "v196", "v197", "v198", "v199", "v200", "v201", "v202", "v203", "v240", "v241", "v242", "v243", "v244", "v245", "v246", "v247", "v248", "v249", "v250", "v251", "s40", "s41", "s42", "scc", "memory");
}
DI int fresh(int v) { asm volatile("" : "+v"(v)); return v; }
DI float max16(const f32x16& s, float m) {
#pragma unroll
  for (int i = 0; i < 16; i += 2) m = fmaxf(fmaxf(s[i], s[i + 1]), m);
  return m;
}

__global__ __launch_bounds__(512, 2) void fused_block(const u16* __restrict__ x, const u16* __restrict__ wqkv, const u16* __restrict__ wo,
                                                      const u16* __restrict__ w1, const u16* __restrict__ b1, const u16* __restrict__ w2,
                                                      const u16* __restrict__ b2, u16* __restrict__ out, char* __restrict__ ws) {
  int tid = threadIdx.x, lane = tid & 63;
  const int w = __builtin_amdgcn_readfirstlane(tid >> 6), m = w & 3;
  const int b = blockIdx.x, xc = b & 7, idx = b >> 3;
  const int rb = 4 * (xc >> 1) + (idx & 3), cg = 8 * (xc & 1) + (idx >> 2);
  const int head = 4 * xc + (idx & 3), qb = idx >> 2;
  const int rt = 4 * xc + (idx & 3), ct = idx >> 2;
  const bool ffn = cg < 14;
  int t = lane & 31, h = lane >> 5, trq, trc;
#define RELANE() do { tid = fresh(tid); lane = tid & 63; t = lane & 31; h = lane >> 5; trq = (lane & 15) >> 2; trc = ((lane >> 4) & 1) * 32 + (lane & 3) * 8; } while (0)
#define KVSRC(i) ((((i) + 2 * qb) & 15) * 8192)
#define ROWSTATS() do { if (__builtin_expect(!__all(rsw[1] == (unsigned)tag), 0)) { \
      _Pragma("nounroll") for (unsigned spin = 0; spin < (1u << 18); spin++) { \
        const u64 v = __hip_atomic_load((GLBP u64*)(ws + rso), __ATOMIC_RELAXED, __HIP_MEMORY_SCOPE_AGENT); \
        rsw = (u32x2){(unsigned)v, (unsigned)(v >> 32)}; \
        if (__all(rsw[1] == (unsigned)tag)) break; } } \
    const unsigned rv = rsw[0]; ra = __builtin_amdgcn_rcpf(__uint_as_float(rv & 0xffff0000u)); \
    rowf = __builtin_bit_cast(bf16x8, (u32x4){(rv & 0xffffu) | (rv << 16), rv >> 16, 0u, 0u}); } while (0)
  unsigned pf0 = 0; u32x4 xrow;
  u64 tag;
  asm volatile("s_load_dwordx2 %0, %1, 0x0" : "=s"(tag) : "s"(ws + WS_EPOCH) : "memory");
  const int cf = ffn ? cg : 13, xr = idx >> 2, wr = idx & 3;
  const char* xb = (const char*)x + (size_t)(rb * 128 + 16 * w) * 1024;
  const unsigned xv = h * 1024 + ((t ^ h) << 4), xm = L_X + w * 8192;
#pragma unroll
  for (int i = 0; i < 8; i++) { const unsigned ii = (i + xr) & 7; dma16(xb, xv ^ (ii * 0x820), xm + ii * 1024); }
  if (w == 0) {
    const char* pc; asm volatile("s_getpc_b64 %0" : "=s"(pc));
    asm volatile("global_load_dword %0, %1, %2" : "=v"(pf0) : "v"((unsigned)(idx * 11 + (lane < 11 ? lane : 0)) * 128u), "s"(pc) : "memory");
  }
  unsigned qo[3], qm[3], gv[2], gm[2];
#pragma unroll
  for (int k = 0; k < 3; k++) {
    const int f = __builtin_amdgcn_readfirstlane((k + wr) % 3), c = 64 * f + lane, r = c / 12;
    qo[k] = r * 3072 + (c - r * 12) * 16; qm[k] = L_QW + w * 3072 + f * 1024;
  }
#pragma unroll
  for (int k = 0; k < 2; k++) { gv[k] = ((k ^ wr) & 1) * 0x60000; gm[k] = ((k ^ wr) & 1) * 24576; }
  const char* wqb = (const char*)wqkv + cg * 192 + (size_t)w * (16 * 3072);
  __builtin_amdgcn_s_barrier();
  asm volatile("global_load_dwordx4 %0, %1, %2" : "=v"(xrow) : "v"(lane * 16), "s"((const char*)x + (size_t)(rb * 128 + cg * 8 + w) * 1024) : "memory");
#pragma unroll
  for (int j = 0; j < 4; j++) dma16(wqb, qo[j % 3] + gv[j / 3], qm[j % 3] + gm[j / 3]);
  const unsigned one = 0x3f803f80u, msk = 0xffff0000u;
  if (w == 0) VMCNT(6); else VMCNT(5);
  __builtin_amdgcn_s_barrier();
  dma16(wqb, qo[1] + gv[1], qm[1] + gm[1]); dma16(wqb, qo[2] + gv[1], qm[2] + gm[1]);
  asm volatile("s_waitcnt vmcnt(6) lgkmcnt(0)" : "+v"(xrow), "+v"(pf0), "+s"(tag) :: "memory");
  tag += 1;
  {
    float rs = 0.f, rq = 0.f;
#pragma unroll
    for (int j = 0; j < 4; j++) {
      const unsigned xj = xrow[j];
      rs = __builtin_amdgcn_fdot2_f32_bf16(__builtin_bit_cast(bf16x2, xj), __builtin_bit_cast(bf16x2, one), rs, false);
      rq = __builtin_amdgcn_fdot2_f32_bf16(__builtin_bit_cast(bf16x2, xj), __builtin_bit_cast(bf16x2, xj), rq, false);
    }
    float v = half_pick(rs, rq);
    v += dpp<0x111, 0xf>(v); v += dpp<0x112, 0xf>(v); v += dpp<0x114, 0xf>(v); v += dpp<0x118, 0xf>(v);
    v += dpp<0x142, 0xa>(v);
    const float mean = __builtin_bit_cast(float, __builtin_amdgcn_readlane(__builtin_bit_cast(int, v), 31)) * (1.0f / 512);
    const float var = fmaxf(__builtin_bit_cast(float, __builtin_amdgcn_readlane(__builtin_bit_cast(int, v), 63)) * (1.0f / 512) - mean * mean, 0.f);
    const unsigned val = pk2(-mean, __builtin_amdgcn_sqrtf(rbf(rbf(var) + 1e-5f)));
    if (lane == 0) put_flag(ws, WS_RS + (rb * 128 + cg * 8 + w) * 8, (tag << 32) | val);
  }
  if (w < 4) {
    RELANE();
    unsigned xa[8], t0, t1;
#pragma unroll
    for (int i = 0; i < 8; i++) xa[i] = L_X + (32 * m + t) * 512 + ((((i ^ m) << 1) ^ h ^ (t & 15)) << 4);
    f32x16 X[8];
    asm volatile(
      "ds_read_b128 v[128:131], %[a0] offset:0\n"
      "ds_read_b128 v[132:135], %[a1] offset:0\n"
      "ds_read_b128 v[136:139], %[a2] offset:0\n"
      "ds_read_b128 v[140:143], %[a3] offset:0\n"
      "ds_read_b128 v[144:147], %[a4] offset:0\n"
      "ds_read_b128 v[148:151], %[a5] offset:0\n"
      "ds_read_b128 v[152:155], %[a6] offset:0\n"
      "ds_read_b128 v[156:159], %[a7] offset:0\n"
      "ds_read_b128 v[160:163], %[a0] offset:256\n"
      "ds_read_b128 v[164:167], %[a1] offset:256\n"
      "ds_read_b128 v[168:171], %[a2] offset:256\n"
      "ds_read_b128 v[172:175], %[a3] offset:256\n"
      "ds_read_b128 v[176:179], %[a4] offset:256\n"
      "ds_read_b128 v[180:183], %[a5] offset:256\n"
      "ds_read_b128 v[184:187], %[a6] offset:256\n"
      "ds_read_b128 v[188:191], %[a7] offset:256\n"
      "s_waitcnt lgkmcnt(0)\n"
        : "={v[128:143]}"(X[0]), "={v[144:159]}"(X[1]), "={v[160:175]}"(X[2]), "={v[176:191]}"(X[3]) : XADR : "memory");
    BAR();
    const int tile = rb * 4 + m;
    const char* w1b = (const char*)w1 + cf * 256;
    const unsigned fq = WS_FQ + (cg * 16 + rb) * 8, rso = WS_RS + (rb * 128 + 32 * m + t) * 8;
    const char* kvp = ws + WS_KV + (size_t)head * 131072;
    float ra; bf16x8 rowf;
    u32x2 rsw;
    RELANE();
    unsigned wb[4];
#pragma unroll
    for (int i = 0; i < 4; i++) wb[i] = L_QW + ((i ^ m) * 16 + 8 * h + trq) * 192 + trc;
    f32x16 a0, a1, a2;
    float c0 = 0.f, c1 = 0.f, c2 = 0.f;
    __builtin_amdgcn_s_setprio(2);
    {
      const unsigned ob = WS_O + ((qb * 8 + 2 * w) * 32 + head) * 1024 + lane * 16;
      const u32x4 un = {~0u, ~0u, ~0u, ~0u};
      gstore_sc(ws, ob, un); gstore_sc(ws, ob + 32768, un);
    }
    VMCNT(2);
    BAR();
    asm volatile(
      "s_cmp_gt_u32 %[cg], 10\n"
      "s_cbranch_scc1 2f\n"
      "s_cmp_eq_u32 %[cg], 10\n"
      "s_cbranch_scc1 1f\n"
      "ds_read_b64_tr_b16 v[48:49], %[b0] offset:0\n"
      "ds_read_b64_tr_b16 v[50:51], %[b0] offset:768\n"
      "ds_read_b64_tr_b16 v[52:53], %[b0] offset:64\n"
      "ds_read_b64_tr_b16 v[54:55], %[b0] offset:832\n"
      "ds_read_b64_tr_b16 v[56:57], %[b0] offset:128\n"
      "ds_read_b64_tr_b16 v[58:59], %[b0] offset:896\n"
      "ds_read_b64_tr_b16 v[60:61], %[b1] offset:0\n"
      "ds_read_b64_tr_b16 v[62:63], %[b1] offset:768\n"
      "ds_read_b64_tr_b16 v[64:65], %[b1] offset:64\n"
      "ds_read_b64_tr_b16 v[66:67], %[b1] offset:832\n"
      "ds_read_b64_tr_b16 v[68:69], %[b1] offset:128\n"
      "ds_read_b64_tr_b16 v[70:71], %[b1] offset:896\n"
      "ds_read_b64_tr_b16 v[72:73], %[b2] offset:0\n"
      "ds_read_b64_tr_b16 v[74:75], %[b2] offset:768\n"
      "s_waitcnt lgkmcnt(10)\n"
      "v_mfma_f32_32x32x16_bf16 v[80:95], v[48:51], v[128:131], 0\n"
      "ds_read_b64_tr_b16 v[76:77], %[b2] offset:64\n"
      "ds_read_b64_tr_b16 v[78:79], %[b2] offset:832\n"
      "v_lshlrev_b32 %[t0], 16, v48\n"
      "v_and_b32 %[t1], %[msk], v48\n"
      "v_add_f32 %[c0], %[c0], %[t0]\n"
      "v_add_f32 %[c0], %[c0], %[t1]\n"
      "v_mfma_f32_32x32x16_bf16 v[96:111], v[52:55], v[128:131], 0\n"
      "ds_read_b64_tr_b16 v[48:49], %[b2] offset:128\n"
      "ds_read_b64_tr_b16 v[50:51], %[b2] offset:896\n"
      "v_lshlrev_b32 %[t0], 16, v52\n"
      "v_and_b32 %[t1], %[msk], v52\n"
      "v_add_f32 %[c1], %[c1], %[t0]\n"
      "v_add_f32 %[c1], %[c1], %[t1]\n"
      "s_waitcnt lgkmcnt(10)\n"
      "v_mfma_f32_32x32x16_bf16 v[112:127], v[56:59], v[128:131], 0\n"
      "ds_read_b64_tr_b16 v[52:53], %[b3] offset:0\n"
      "ds_read_b64_tr_b16 v[54:55], %[b3] offset:768\n"
      "v_lshlrev_b32 %[t0], 16, v56\n"
      "v_and_b32 %[t1], %[msk], v56\n"
      "v_add_f32 %[c2], %[c2], %[t0]\n"
      "v_add_f32 %[c2], %[c2], %[t1]\n"
      "v_mfma_f32_32x32x16_bf16 v[80:95], v[60:63], v[132:135], v[80:95]\n"
      "ds_read_b64_tr_b16 v[56:57], %[b3] offset:64\n"
      "ds_read_b64_tr_b16 v[58:59], %[b3] offset:832\n"
      "v_lshlrev_b32 %[t0], 16, v61\n"
      "v_and_b32 %[t1], %[msk], v61\n"
      "v_add_f32 %[c0], %[c0], %[t0]\n"
      "v_add_f32 %[c0], %[c0], %[t1]\n"
      "s_waitcnt lgkmcnt(10)\n"
      "v_mfma_f32_32x32x16_bf16 v[96:111], v[64:67], v[132:135], v[96:111]\n"
      "ds_read_b64_tr_b16 v[60:61], %[b3] offset:128\n"
      "ds_read_b64_tr_b16 v[62:63], %[b3] offset:896\n"
      "v_lshlrev_b32 %[t0], 16, v65\n"
      "v_and_b32 %[t1], %[msk], v65\n"
      "v_add_f32 %[c1], %[c1], %[t0]\n"
      "v_add_f32 %[c1], %[c1], %[t1]\n"
      "v_mfma_f32_32x32x16_bf16 v[112:127], v[68:71], v[132:135], v[112:127]\n"
      "ds_read_b64_tr_b16 v[64:65], %[b0] offset:12288\n"
      "ds_read_b64_tr_b16 v[66:67], %[b0] offset:13056\n"
      "v_lshlrev_b32 %[t0], 16, v69\n"
      "v_and_b32 %[t1], %[msk], v69\n"
      "v_add_f32 %[c2], %[c2], %[t0]\n"
      "v_add_f32 %[c2], %[c2], %[t1]\n"
      "s_waitcnt lgkmcnt(10)\n"
      "v_mfma_f32_32x32x16_bf16 v[80:95], v[72:75], v[136:139], v[80:95]\n"
      "ds_read_b64_tr_b16 v[68:69], %[b0] offset:12352\n"
      "ds_read_b64_tr_b16 v[70:71], %[b0] offset:13120\n"
      "v_lshlrev_b32 %[t0], 16, v74\n"
      "v_and_b32 %[t1], %[msk], v74\n"
      "v_add_f32 %[c0], %[c0], %[t0]\n"
      "v_add_f32 %[c0], %[c0], %[t1]\n"
      "v_mfma_f32_32x32x16_bf16 v[96:111], v[76:79], v[136:139], v[96:111]\n"
      "ds_read_b64_tr_b16 v[72:73], %[b0] offset:12416\n"
      "ds_read_b64_tr_b16 v[74:75], %[b0] offset:13184\n"
      "v_lshlrev_b32 %[t0], 16, v78\n"
      "v_and_b32 %[t1], %[msk], v78\n"
      "v_add_f32 %[c1], %[c1], %[t0]\n"
      "v_add_f32 %[c1], %[c1], %[t1]\n"
      "s_waitcnt lgkmcnt(10)\n"
      "v_mfma_f32_32x32x16_bf16 v[112:127], v[48:51], v[136:139], v[112:127]\n"
      "ds_read_b64_tr_b16 v[76:77], %[b1] offset:12288\n"
      "ds_read_b64_tr_b16 v[78:79], %[b1] offset:13056\n"
      "v_lshlrev_b32 %[t0], 16, v50\n"
      "v_and_b32 %[t1], %[msk], v50\n"
      "v_add_f32 %[c2], %[c2], %[t0]\n"
      "v_add_f32 %[c2], %[c2], %[t1]\n"
      "v_mfma_f32_32x32x16_bf16 v[80:95], v[52:55], v[140:143], v[80:95]\n"
      "ds_read_b64_tr_b16 v[48:49], %[b1] offset:12352\n"
      "ds_read_b64_tr_b16 v[50:51], %[b1] offset:13120\n"
      "v_lshlrev_b32 %[t0], 16, v55\n"
      "v_and_b32 %[t1], %[msk], v55\n"
      "v_add_f32 %[c0], %[c0], %[t0]\n"
      "v_add_f32 %[c0], %[c0], %[t1]\n"
      "s_waitcnt lgkmcnt(10)\n"
      "v_mfma_f32_32x32x16_bf16 v[96:111], v[56:59], v[140:143], v[96:111]\n"
      "ds_read_b64_tr_b16 v[52:53], %[b1] offset:12416\n"
      "ds_read_b64_tr_b16 v[54:55], %[b1] offset:13184\n"
      "v_lshlrev_b32 %[t0], 16, v59\n"
      "v_and_b32 %[t1], %[msk], v59\n"
      "v_add_f32 %[c1], %[c1], %[t0]\n"
      "v_add_f32 %[c1], %[c1], %[t1]\n"
      "v_mfma_f32_32x32x16_bf16 v[112:127], v[60:63], v[140:143], v[112:127]\n"
      "ds_read_b64_tr_b16 v[56:57], %[b2] offset:12288\n"
      "ds_read_b64_tr_b16 v[58:59], %[b2] offset:13056\n"
      "v_lshlrev_b32 %[t0], 16, v63\n"
      "v_and_b32 %[t1], %[msk], v63\n"
      "v_add_f32 %[c2], %[c2], %[t0]\n"
      "v_add_f32 %[c2], %[c2], %[t1]\n"
      "s_waitcnt lgkmcnt(10)\n"
      "v_mfma_f32_32x32x16_bf16 v[80:95], v[64:67], v[144:147], v[80:95]\n"
      "ds_read_b64_tr_b16 v[60:61], %[b2] offset:12352\n"
      "ds_read_b64_tr_b16 v[62:63], %[b2] offset:13120\n"
      "v_lshlrev_b32 %[t0], 16, v64\n"
      "v_and_b32 %[t1], %[msk], v64\n"
      "v_add_f32 %[c0], %[c0], %[t0]\n"
      "v_add_f32 %[c0], %[c0], %[t1]\n"
      "v_mfma_f32_32x32x16_bf16 v[96:111], v[68:71], v[144:147], v[96:111]\n"
      "ds_read_b64_tr_b16 v[64:65], %[b2] offset:12416\n"
      "ds_read_b64_tr_b16 v[66:67], %[b2] offset:13184\n"
      "v_lshlrev_b32 %[t0], 16, v68\n"
      "v_and_b32 %[t1], %[msk], v68\n"
      "v_add_f32 %[c1], %[c1], %[t0]\n"
      "v_add_f32 %[c1], %[c1], %[t1]\n"
      "s_waitcnt lgkmcnt(10)\n"
      "v_mfma_f32_32x32x16_bf16 v[112:127], v[72:75], v[144:147], v[112:127]\n"
      "ds_read_b64_tr_b16 v[68:69], %[b3] offset:12288\n"
      "ds_read_b64_tr_b16 v[70:71], %[b3] offset:13056\n"
      "v_lshlrev_b32 %[t0], 16, v72\n"
      "v_and_b32 %[t1], %[msk], v72\n"
      "v_add_f32 %[c2], %[c2], %[t0]\n"
      "v_add_f32 %[c2], %[c2], %[t1]\n"
      "v_mfma_f32_32x32x16_bf16 v[80:95], v[76:79], v[148:151], v[80:95]\n"
      "ds_read_b64_tr_b16 v[72:73], %[b3] offset:12352\n"
      "ds_read_b64_tr_b16 v[74:75], %[b3] offset:13120\n"
      "v_lshlrev_b32 %[t0], 16, v77\n"
      "v_and_b32 %[t1], %[msk], v77\n"
      "v_add_f32 %[c0], %[c0], %[t0]\n"
      "v_add_f32 %[c0], %[c0], %[t1]\n"
      "s_waitcnt lgkmcnt(10)\n"
      "v_mfma_f32_32x32x16_bf16 v[96:111], v[48:51], v[148:151], v[96:111]\n"
      "ds_read_b64_tr_b16 v[76:77], %[b3] offset:12416\n"
      "ds_read_b64_tr_b16 v[78:79], %[b3] offset:13184\n"
      "v_lshlrev_b32 %[t0], 16, v49\n"
      "v_and_b32 %[t1], %[msk], v49\n"
      "v_add_f32 %[c1], %[c1], %[t0]\n"
      "v_add_f32 %[c1], %[c1], %[t1]\n"
      "v_mfma_f32_32x32x16_bf16 v[112:127], v[52:55], v[148:151], v[112:127]\n"
      "ds_read_b64_tr_b16 v[48:49], %[b0] offset:24576\n"
      "ds_read_b64_tr_b16 v[50:51], %[b0] offset:25344\n"
      "v_lshlrev_b32 %[t0], 16, v53\n"
      "v_and_b32 %[t1], %[msk], v53\n"
      "v_add_f32 %[c2], %[c2], %[t0]\n"
      "v_add_f32 %[c2], %[c2], %[t1]\n"
      "s_waitcnt lgkmcnt(10)\n"
      "v_mfma_f32_32x32x16_bf16 v[80:95], v[56:59], v[152:155], v[80:95]\n"
      "ds_read_b64_tr_b16 v[52:53], %[b0] offset:24640\n"
      "ds_read_b64_tr_b16 v[54:55], %[b0] offset:25408\n"
      "v_lshlrev_b32 %[t0], 16, v58\n"
      "v_and_b32 %[t1], %[msk], v58\n"
      "v_add_f32 %[c0], %[c0], %[t0]\n"
      "v_add_f32 %[c0], %[c0], %[t1]\n"
      "v_mfma_f32_32x32x16_bf16 v[96:111], v[60:63], v[152:155], v[96:111]\n"
      "ds_read_b64_tr_b16 v[56:57], %[b0] offset:24704\n"
      "ds_read_b64_tr_b16 v[58:59], %[b0] offset:25472\n"
      "v_lshlrev_b32 %[t0], 16, v62\n"
      "v_and_b32 %[t1], %[msk], v62\n"
      "v_add_f32 %[c1], %[c1], %[t0]\n"
      "v_add_f32 %[c1], %[c1], %[t1]\n"
      "s_waitcnt lgkmcnt(10)\n"
      "v_mfma_f32_32x32x16_bf16 v[112:127], v[64:67], v[152:155], v[112:127]\n"
      "ds_read_b64_tr_b16 v[60:61], %[b1] offset:24576\n"
      "ds_read_b64_tr_b16 v[62:63], %[b1] offset:25344\n"
      "v_lshlrev_b32 %[t0], 16, v66\n"
      "v_and_b32 %[t1], %[msk], v66\n"
      "v_add_f32 %[c2], %[c2], %[t0]\n"
      "v_add_f32 %[c2], %[c2], %[t1]\n"
      "v_mfma_f32_32x32x16_bf16 v[80:95], v[68:71], v[156:159], v[80:95]\n"
      "ds_read_b64_tr_b16 v[64:65], %[b1] offset:24640\n"
      "ds_read_b64_tr_b16 v[66:67], %[b1] offset:25408\n"
      "v_lshlrev_b32 %[t0], 16, v71\n"
      "v_and_b32 %[t1], %[msk], v71\n"
      "v_add_f32 %[c0], %[c0], %[t0]\n"
      "v_add_f32 %[c0], %[c0], %[t1]\n"
      "s_waitcnt lgkmcnt(10)\n"
      "v_mfma_f32_32x32x16_bf16 v[96:111], v[72:75], v[156:159], v[96:111]\n"
      "ds_read_b64_tr_b16 v[68:69], %[b1] offset:24704\n"
      "ds_read_b64_tr_b16 v[70:71], %[b1] offset:25472\n"
      "v_lshlrev_b32 %[t0], 16, v75\n"
      "v_and_b32 %[t1], %[msk], v75\n"
      "v_add_f32 %[c1], %[c1], %[t0]\n"
      "v_add_f32 %[c1], %[c1], %[t1]\n"
      "v_mfma_f32_32x32x16_bf16 v[112:127], v[76:79], v[156:159], v[112:127]\n"
      "ds_read_b64_tr_b16 v[72:73], %[b2] offset:24576\n"
      "ds_read_b64_tr_b16 v[74:75], %[b2] offset:25344\n"
      "v_lshlrev_b32 %[t0], 16, v79\n"
      "v_and_b32 %[t1], %[msk], v79\n"
      "v_add_f32 %[c2], %[c2], %[t0]\n"
      "v_add_f32 %[c2], %[c2], %[t1]\n"
      "s_waitcnt lgkmcnt(10)\n"
      "v_mfma_f32_32x32x16_bf16 v[80:95], v[48:51], v[160:163], v[80:95]\n"
      "ds_read_b64_tr_b16 v[76:77], %[b2] offset:24640\n"
      "ds_read_b64_tr_b16 v[78:79], %[b2] offset:25408\n"
      "v_lshlrev_b32 %[t0], 16, v48\n"
      "v_and_b32 %[t1], %[msk], v48\n"
      "v_add_f32 %[c0], %[c0], %[t0]\n"
      "v_add_f32 %[c0], %[c0], %[t1]\n"
      "v_mfma_f32_32x32x16_bf16 v[96:111], v[52:55], v[160:163], v[96:111]\n"
      "ds_read_b64_tr_b16 v[48:49], %[b2] offset:24704\n"
      "ds_read_b64_tr_b16 v[50:51], %[b2] offset:25472\n"
      "v_lshlrev_b32 %[t0], 16, v52\n"
      "v_and_b32 %[t1], %[msk], v52\n"
      "v_add_f32 %[c1], %[c1], %[t0]\n"
      "v_add_f32 %[c1], %[c1], %[t1]\n"
      "s_waitcnt lgkmcnt(10)\n"
      "v_mfma_f32_32x32x16_bf16 v[112:127], v[56:59], v[160:163], v[112:127]\n"
      "ds_read_b64_tr_b16 v[52:53], %[b3] offset:24576\n"
      "ds_read_b64_tr_b16 v[54:55], %[b3] offset:25344\n"
      "v_lshlrev_b32 %[t0], 16, v56\n"
      "v_and_b32 %[t1], %[msk], v56\n"
      "v_add_f32 %[c2], %[c2], %[t0]\n"
      "v_add_f32 %[c2], %[c2], %[t1]\n"
      "v_mfma_f32_32x32x16_bf16 v[80:95], v[60:63], v[164:167], v[80:95]\n"
      "ds_read_b64_tr_b16 v[56:57], %[b3] offset:24640\n"
      "ds_read_b64_tr_b16 v[58:59], %[b3] offset:25408\n"
      "v_lshlrev_b32 %[t0], 16, v61\n"
      "v_and_b32 %[t1], %[msk], v61\n"
      "v_add_f32 %[c0], %[c0], %[t0]\n"
      "v_add_f32 %[c0], %[c0], %[t1]\n"
      "s_waitcnt lgkmcnt(10)\n"
      "v_mfma_f32_32x32x16_bf16 v[96:111], v[64:67], v[164:167], v[96:111]\n"
      "ds_read_b64_tr_b16 v[60:61], %[b3] offset:24704\n"
      "ds_read_b64_tr_b16 v[62:63], %[b3] offset:25472\n"
      "v_lshlrev_b32 %[t0], 16, v65\n"
      "v_and_b32 %[t1], %[msk], v65\n"
      "v_add_f32 %[c1], %[c1], %[t0]\n"
      "v_add_f32 %[c1], %[c1], %[t1]\n"
      "v_mfma_f32_32x32x16_bf16 v[112:127], v[68:71], v[164:167], v[112:127]\n"
      "v_lshlrev_b32 %[t0], 16, v69\n"
      "v_and_b32 %[t1], %[msk], v69\n"
      "v_add_f32 %[c2], %[c2], %[t0]\n"
      "v_add_f32 %[c2], %[c2], %[t1]\n"
      "s_waitcnt lgkmcnt(8)\n"
      "v_mfma_f32_32x32x16_bf16 v[80:95], v[72:75], v[168:171], v[80:95]\n"
      "v_lshlrev_b32 %[t0], 16, v74\n"
      "v_and_b32 %[t1], %[msk], v74\n"
      "v_add_f32 %[c0], %[c0], %[t0]\n"
      "v_add_f32 %[c0], %[c0], %[t1]\n"
      "v_mfma_f32_32x32x16_bf16 v[96:111], v[76:79], v[168:171], v[96:111]\n"
      "v_lshlrev_b32 %[t0], 16, v78\n"
      "v_and_b32 %[t1], %[msk], v78\n"
      "v_add_f32 %[c1], %[c1], %[t0]\n"
      "v_add_f32 %[c1], %[c1], %[t1]\n"
      "s_waitcnt lgkmcnt(4)\n"
      "v_mfma_f32_32x32x16_bf16 v[112:127], v[48:51], v[168:171], v[112:127]\n"
      "v_lshlrev_b32 %[t0], 16, v50\n"
      "v_and_b32 %[t1], %[msk], v50\n"
      "v_add_f32 %[c2], %[c2], %[t0]\n"
      "v_add_f32 %[c2], %[c2], %[t1]\n"
      "v_mfma_f32_32x32x16_bf16 v[80:95], v[52:55], v[172:175], v[80:95]\n"
      "v_lshlrev_b32 %[t0], 16, v55\n"
      "v_and_b32 %[t1], %[msk], v55\n"
      "v_add_f32 %[c0], %[c0], %[t0]\n"
      "v_add_f32 %[c0], %[c0], %[t1]\n"
      "s_waitcnt lgkmcnt(0)\n"
      "v_mfma_f32_32x32x16_bf16 v[96:111], v[56:59], v[172:175], v[96:111]\n"
      "v_lshlrev_b32 %[t0], 16, v59\n"
      "v_and_b32 %[t1], %[msk], v59\n"
      "v_add_f32 %[c1], %[c1], %[t0]\n"
      "v_add_f32 %[c1], %[c1], %[t1]\n"
      "v_mfma_f32_32x32x16_bf16 v[112:127], v[60:63], v[172:175], v[112:127]\n"
      "v_lshlrev_b32 %[t0], 16, v63\n"
      "v_and_b32 %[t1], %[msk], v63\n"
      "v_add_f32 %[c2], %[c2], %[t0]\n"
      "v_add_f32 %[c2], %[c2], %[t1]\n"
      "s_nop 15\n"
      "s_branch 3f\n"
      "1:\n"
      "ds_read_b64_tr_b16 v[48:49], %[b0] offset:0\n"
      "ds_read_b64_tr_b16 v[50:51], %[b0] offset:768\n"
      "ds_read_b64_tr_b16 v[52:53], %[b0] offset:64\n"
      "ds_read_b64_tr_b16 v[54:55], %[b0] offset:832\n"
      "ds_read_b64_tr_b16 v[56:57], %[b0] offset:128\n"
      "ds_read_b64_tr_b16 v[58:59], %[b0] offset:896\n"
      "ds_read_b64_tr_b16 v[60:61], %[b1] offset:0\n"
      "ds_read_b64_tr_b16 v[62:63], %[b1] offset:768\n"
      "ds_read_b64_tr_b16 v[64:65], %[b1] offset:64\n"
      "ds_read_b64_tr_b16 v[66:67], %[b1] offset:832\n"
      "ds_read_b64_tr_b16 v[68:69], %[b1] offset:128\n"
      "ds_read_b64_tr_b16 v[70:71], %[b1] offset:896\n"
      "ds_read_b64_tr_b16 v[72:73], %[b2] offset:0\n"
      "ds_read_b64_tr_b16 v[74:75], %[b2] offset:768\n"
      "s_waitcnt lgkmcnt(10)\n"
      "v_mfma_f32_32x32x16_bf16 v[80:95], v[48:51], v[128:131], 0\n"
      "ds_read_b64_tr_b16 v[76:77], %[b2] offset:64\n"
      "ds_read_b64_tr_b16 v[78:79], %[b2] offset:832\n"
      "v_lshlrev_b32 %[t0], 16, v48\n"
      "v_and_b32 %[t1], %[msk], v48\n"
      "v_add_f32 %[c0], %[c0], %[t0]\n"
      "v_add_f32 %[c0], %[c0], %[t1]\n"
      "v_mfma_f32_32x32x16_bf16 v[96:111], v[52:55], v[128:131], 0\n"
      "ds_read_b64_tr_b16 v[48:49], %[b2] offset:128\n"
      "ds_read_b64_tr_b16 v[50:51], %[b2] offset:896\n"
      "v_lshlrev_b32 %[t0], 16, v52\n"
      "v_and_b32 %[t1], %[msk], v52\n"
      "v_add_f32 %[c1], %[c1], %[t0]\n"
      "v_add_f32 %[c1], %[c1], %[t1]\n"
      "s_waitcnt lgkmcnt(10)\n"
      "v_mfma_f32_32x32x16_bf16 v[112:127], v[128:131], v[56:59], 0\n"
      "ds_read_b64_tr_b16 v[52:53], %[b3] offset:0\n"
      "ds_read_b64_tr_b16 v[54:55], %[b3] offset:768\n"
      "v_lshlrev_b32 %[t0], 16, v56\n"
      "v_and_b32 %[t1], %[msk], v56\n"
      "v_add_f32 %[c2], %[c2], %[t0]\n"
      "v_add_f32 %[c2], %[c2], %[t1]\n"
      "v_mfma_f32_32x32x16_bf16 v[80:95], v[60:63], v[132:135], v[80:95]\n"
      "ds_read_b64_tr_b16 v[56:57], %[b3] offset:64\n"
      "ds_read_b64_tr_b16 v[58:59], %[b3] offset:832\n"
      "v_lshlrev_b32 %[t0], 16, v61\n"
      "v_and_b32 %[t1], %[msk], v61\n"
      "v_add_f32 %[c0], %[c0], %[t0]\n"
      "v_add_f32 %[c0], %[c0], %[t1]\n"
      "s_waitcnt lgkmcnt(10)\n"
      "v_mfma_f32_32x32x16_bf16 v[96:111], v[64:67], v[132:135], v[96:111]\n"
      "ds_read_b64_tr_b16 v[60:61], %[b3] offset:128\n"
      "ds_read_b64_tr_b16 v[62:63], %[b3] offset:896\n"
      "v_lshlrev_b32 %[t0], 16, v65\n"
      "v_and_b32 %[t1], %[msk], v65\n"
      "v_add_f32 %[c1], %[c1], %[t0]\n"
      "v_add_f32 %[c1], %[c1], %[t1]\n"
      "v_mfma_f32_32x32x16_bf16 v[112:127], v[132:135], v[68:71], v[112:127]\n"
      "ds_read_b64_tr_b16 v[64:65], %[b0] offset:12288\n"
      "ds_read_b64_tr_b16 v[66:67], %[b0] offset:13056\n"
      "v_lshlrev_b32 %[t0], 16, v69\n"
      "v_and_b32 %[t1], %[msk], v69\n"
      "v_add_f32 %[c2], %[c2], %[t0]\n"
      "v_add_f32 %[c2], %[c2], %[t1]\n"
      "s_waitcnt lgkmcnt(10)\n"
      "v_mfma_f32_32x32x16_bf16 v[80:95], v[72:75], v[136:139], v[80:95]\n"
      "ds_read_b64_tr_b16 v[68:69], %[b0] offset:12352\n"
      "ds_read_b64_tr_b16 v[70:71], %[b0] offset:13120\n"
      "v_lshlrev_b32 %[t0], 16, v74\n"
      "v_and_b32 %[t1], %[msk], v74\n"
      "v_add_f32 %[c0], %[c0], %[t0]\n"
      "v_add_f32 %[c0], %[c0], %[t1]\n"
      "v_mfma_f32_32x32x16_bf16 v[96:111], v[76:79], v[136:139], v[96:111]\n"
      "ds_read_b64_tr_b16 v[72:73], %[b0] offset:12416\n"
      "ds_read_b64_tr_b16 v[74:75], %[b0] offset:13184\n"
      "v_lshlrev_b32 %[t0], 16, v78\n"
      "v_and_b32 %[t1], %[msk], v78\n"
      "v_add_f32 %[c1], %[c1], %[t0]\n"
      "v_add_f32 %[c1], %[c1], %[t1]\n"
      "s_waitcnt lgkmcnt(10)\n"
      "v_mfma_f32_32x32x16_bf16 v[112:127], v[136:139], v[48:51], v[112:127]\n"
      "ds_read_b64_tr_b16 v[76:77], %[b1] offset:12288\n"
      "ds_read_b64_tr_b16 v[78:79], %[b1] offset:13056\n"
      "v_lshlrev_b32 %[t0], 16, v50\n"
      "v_and_b32 %[t1], %[msk], v50\n"
      "v_add_f32 %[c2], %[c2], %[t0]\n"
      "v_add_f32 %[c2], %[c2], %[t1]\n"
      "v_mfma_f32_32x32x16_bf16 v[80:95], v[52:55], v[140:143], v[80:95]\n"
      "ds_read_b64_tr_b16 v[48:49], %[b1] offset:12352\n"
      "ds_read_b64_tr_b16 v[50:51], %[b1] offset:13120\n"
      "v_lshlrev_b32 %[t0], 16, v55\n"
      "v_and_b32 %[t1], %[msk], v55\n"
      "v_add_f32 %[c0], %[c0], %[t0]\n"
      "v_add_f32 %[c0], %[c0], %[t1]\n"
      "s_waitcnt lgkmcnt(10)\n"
      "v_mfma_f32_32x32x16_bf16 v[96:111], v[56:59], v[140:143], v[96:111]\n"
      "ds_read_b64_tr_b16 v[52:53], %[b1] offset:12416\n"
      "ds_read_b64_tr_b16 v[54:55], %[b1] offset:13184\n"
      "v_lshlrev_b32 %[t0], 16, v59\n"
      "v_and_b32 %[t1], %[msk], v59\n"
      "v_add_f32 %[c1], %[c1], %[t0]\n"
      "v_add_f32 %[c1], %[c1], %[t1]\n"
      "v_mfma_f32_32x32x16_bf16 v[112:127], v[140:143], v[60:63], v[112:127]\n"
      "ds_read_b64_tr_b16 v[56:57], %[b2] offset:12288\n"
      "ds_read_b64_tr_b16 v[58:59], %[b2] offset:13056\n"
      "v_lshlrev_b32 %[t0], 16, v63\n"
      "v_and_b32 %[t1], %[msk], v63\n"
      "v_add_f32 %[c2], %[c2], %[t0]\n"
      "v_add_f32 %[c2], %[c2], %[t1]\n"
      "s_waitcnt lgkmcnt(10)\n"
      "v_mfma_f32_32x32x16_bf16 v[80:95], v[64:67], v[144:147], v[80:95]\n"
      "ds_read_b64_tr_b16 v[60:61], %[b2] offset:12352\n"
      "ds_read_b64_tr_b16 v[62:63], %[b2] offset:13120\n"
      "v_lshlrev_b32 %[t0], 16, v64\n"
      "v_and_b32 %[t1], %[msk], v64\n"
      "v_add_f32 %[c0], %[c0], %[t0]\n"
      "v_add_f32 %[c0], %[c0], %[t1]\n"
      "v_mfma_f32_32x32x16_bf16 v[96:111], v[68:71], v[144:147], v[96:111]\n"
      "ds_read_b64_tr_b16 v[64:65], %[b2] offset:12416\n"
      "ds_read_b64_tr_b16 v[66:67], %[b2] offset:13184\n"
      "v_lshlrev_b32 %[t0], 16, v68\n"
      "v_and_b32 %[t1], %[msk], v68\n"
      "v_add_f32 %[c1], %[c1], %[t0]\n"
      "v_add_f32 %[c1], %[c1], %[t1]\n"
      "s_waitcnt lgkmcnt(10)\n"
      "v_mfma_f32_32x32x16_bf16 v[112:127], v[144:147], v[72:75], v[112:127]\n"
      "ds_read_b64_tr_b16 v[68:69], %[b3] offset:12288\n"
      "ds_read_b64_tr_b16 v[70:71], %[b3] offset:13056\n"
      "v_lshlrev_b32 %[t0], 16, v72\n"
      "v_and_b32 %[t1], %[msk], v72\n"
      "v_add_f32 %[c2], %[c2], %[t0]\n"
      "v_add_f32 %[c2], %[c2], %[t1]\n"
      "v_mfma_f32_32x32x16_bf16 v[80:95], v[76:79], v[148:151], v[80:95]\n"
      "ds_read_b64_tr_b16 v[72:73], %[b3] offset:12352\n"
      "ds_read_b64_tr_b16 v[74:75], %[b3] offset:13120\n"
      "v_lshlrev_b32 %[t0], 16, v77\n"
      "v_and_b32 %[t1], %[msk], v77\n"
      "v_add_f32 %[c0], %[c0], %[t0]\n"
      "v_add_f32 %[c0], %[c0], %[t1]\n"
      "s_waitcnt lgkmcnt(10)\n"
      "v_mfma_f32_32x32x16_bf16 v[96:111], v[48:51], v[148:151], v[96:111]\n"
      "ds_read_b64_tr_b16 v[76:77], %[b3] offset:12416\n"
      "ds_read_b64_tr_b16 v[78:79], %[b3] offset:13184\n"
      "v_lshlrev_b32 %[t0], 16, v49\n"
      "v_and_b32 %[t1], %[msk], v49\n"
      "v_add_f32 %[c1], %[c1], %[t0]\n"
      "v_add_f32 %[c1], %[c1], %[t1]\n"
      "v_mfma_f32_32x32x16_bf16 v[112:127], v[148:151], v[52:55], v[112:127]\n"
      "ds_read_b64_tr_b16 v[48:49], %[b0] offset:24576\n"
      "ds_read_b64_tr_b16 v[50:51], %[b0] offset:25344\n"
      "v_lshlrev_b32 %[t0], 16, v53\n"
      "v_and_b32 %[t1], %[msk], v53\n"
      "v_add_f32 %[c2], %[c2], %[t0]\n"
      "v_add_f32 %[c2], %[c2], %[t1]\n"
      "s_waitcnt lgkmcnt(10)\n"
      "v_mfma_f32_32x32x16_bf16 v[80:95], v[56:59], v[152:155], v[80:95]\n"
      "ds_read_b64_tr_b16 v[52:53], %[b0] offset:24640\n"
      "ds_read_b64_tr_b16 v[54:55], %[b0] offset:25408\n"
      "v_lshlrev_b32 %[t0], 16, v58\n"
      "v_and_b32 %[t1], %[msk], v58\n"
      "v_add_f32 %[c0], %[c0], %[t0]\n"
      "v_add_f32 %[c0], %[c0], %[t1]\n"
      "v_mfma_f32_32x32x16_bf16 v[96:111], v[60:63], v[152:155], v[96:111]\n"
      "ds_read_b64_tr_b16 v[56:57], %[b0] offset:24704\n"
      "ds_read_b64_tr_b16 v[58:59], %[b0] offset:25472\n"
      "v_lshlrev_b32 %[t0], 16, v62\n"
      "v_and_b32 %[t1], %[msk], v62\n"
      "v_add_f32 %[c1], %[c1], %[t0]\n"
      "v_add_f32 %[c1], %[c1], %[t1]\n"
      "s_waitcnt lgkmcnt(10)\n"
      "v_mfma_f32_32x32x16_bf16 v[112:127], v[152:155], v[64:67], v[112:127]\n"
      "ds_read_b64_tr_b16 v[60:61], %[b1] offset:24576\n"
      "ds_read_b64_tr_b16 v[62:63], %[b1] offset:25344\n"
      "v_lshlrev_b32 %[t0], 16, v66\n"
      "v_and_b32 %[t1], %[msk], v66\n"
      "v_add_f32 %[c2], %[c2], %[t0]\n"
      "v_add_f32 %[c2], %[c2], %[t1]\n"
      "v_mfma_f32_32x32x16_bf16 v[80:95], v[68:71], v[156:159], v[80:95]\n"
      "ds_read_b64_tr_b16 v[64:65], %[b1] offset:24640\n"
      "ds_read_b64_tr_b16 v[66:67], %[b1] offset:25408\n"
      "v_lshlrev_b32 %[t0], 16, v71\n"
      "v_and_b32 %[t1], %[msk], v71\n"
      "v_add_f32 %[c0], %[c0], %[t0]\n"
      "v_add_f32 %[c0], %[c0], %[t1]\n"
      "s_waitcnt lgkmcnt(10)\n"
      "v_mfma_f32_32x32x16_bf16 v[96:111], v[72:75], v[156:159], v[96:111]\n"
      "ds_read_b64_tr_b16 v[68:69], %[b1] offset:24704\n"
      "ds_read_b64_tr_b16 v[70:71], %[b1] offset:25472\n"
      "v_lshlrev_b32 %[t0], 16, v75\n"
      "v_and_b32 %[t1], %[msk], v75\n"
      "v_add_f32 %[c1], %[c1], %[t0]\n"
      "v_add_f32 %[c1], %[c1], %[t1]\n"
      "v_mfma_f32_32x32x16_bf16 v[112:127], v[156:159], v[76:79], v[112:127]\n"
      "ds_read_b64_tr_b16 v[72:73], %[b2] offset:24576\n"
      "ds_read_b64_tr_b16 v[74:75], %[b2] offset:25344\n"
      "v_lshlrev_b32 %[t0], 16, v79\n"
      "v_and_b32 %[t1], %[msk], v79\n"
      "v_add_f32 %[c2], %[c2], %[t0]\n"
      "v_add_f32 %[c2], %[c2], %[t1]\n"
      "s_waitcnt lgkmcnt(10)\n"
      "v_mfma_f32_32x32x16_bf16 v[80:95], v[48:51], v[160:163], v[80:95]\n"
      "ds_read_b64_tr_b16 v[76:77], %[b2] offset:24640\n"
      "ds_read_b64_tr_b16 v[78:79], %[b2] offset:25408\n"
      "v_lshlrev_b32 %[t0], 16, v48\n"
      "v_and_b32 %[t1], %[msk], v48\n"
      "v_add_f32 %[c0], %[c0], %[t0]\n"
      "v_add_f32 %[c0], %[c0], %[t1]\n"
      "v_mfma_f32_32x32x16_bf16 v[96:111], v[52:55], v[160:163], v[96:111]\n"
      "ds_read_b64_tr_b16 v[48:49], %[b2] offset:24704\n"
      "ds_read_b64_tr_b16 v[50:51], %[b2] offset:25472\n"
      "v_lshlrev_b32 %[t0], 16, v52\n"
      "v_and_b32 %[t1], %[msk], v52\n"
      "v_add_f32 %[c1], %[c1], %[t0]\n"
      "v_add_f32 %[c1], %[c1], %[t1]\n"
      "s_waitcnt lgkmcnt(10)\n"
      "v_mfma_f32_32x32x16_bf16 v[112:127], v[160:163], v[56:59], v[112:127]\n"
      "ds_read_b64_tr_b16 v[52:53], %[b3] offset:24576\n"
      "ds_read_b64_tr_b16 v[54:55], %[b3] offset:25344\n"
      "v_lshlrev_b32 %[t0], 16, v56\n"
      "v_and_b32 %[t1], %[msk], v56\n"
      "v_add_f32 %[c2], %[c2], %[t0]\n"
      "v_add_f32 %[c2], %[c2], %[t1]\n"
      "v_mfma_f32_32x32x16_bf16 v[80:95], v[60:63], v[164:167], v[80:95]\n"
      "ds_read_b64_tr_b16 v[56:57], %[b3] offset:24640\n"
      "ds_read_b64_tr_b16 v[58:59], %[b3] offset:25408\n"
      "v_lshlrev_b32 %[t0], 16, v61\n"
      "v_and_b32 %[t1], %[msk], v61\n"
      "v_add_f32 %[c0], %[c0], %[t0]\n"
      "v_add_f32 %[c0], %[c0], %[t1]\n"
      "s_waitcnt lgkmcnt(10)\n"
      "v_mfma_f32_32x32x16_bf16 v[96:111], v[64:67], v[164:167], v[96:111]\n"
      "ds_read_b64_tr_b16 v[60:61], %[b3] offset:24704\n"
      "ds_read_b64_tr_b16 v[62:63], %[b3] offset:25472\n"
      "v_lshlrev_b32 %[t0], 16, v65\n"
      "v_and_b32 %[t1], %[msk], v65\n"
      "v_add_f32 %[c1], %[c1], %[t0]\n"
      "v_add_f32 %[c1], %[c1], %[t1]\n"
      "v_mfma_f32_32x32x16_bf16 v[112:127], v[164:167], v[68:71], v[112:127]\n"
      "v_lshlrev_b32 %[t0], 16, v69\n"
      "v_and_b32 %[t1], %[msk], v69\n"
      "v_add_f32 %[c2], %[c2], %[t0]\n"
      "v_add_f32 %[c2], %[c2], %[t1]\n"
      "s_waitcnt lgkmcnt(8)\n"
      "v_mfma_f32_32x32x16_bf16 v[80:95], v[72:75], v[168:171], v[80:95]\n"
      "v_lshlrev_b32 %[t0], 16, v74\n"
      "v_and_b32 %[t1], %[msk], v74\n"
      "v_add_f32 %[c0], %[c0], %[t0]\n"
      "v_add_f32 %[c0], %[c0], %[t1]\n"
      "v_mfma_f32_32x32x16_bf16 v[96:111], v[76:79], v[168:171], v[96:111]\n"
      "v_lshlrev_b32 %[t0], 16, v78\n"
      "v_and_b32 %[t1], %[msk], v78\n"
      "v_add_f32 %[c1], %[c1], %[t0]\n"
      "v_add_f32 %[c1], %[c1], %[t1]\n"
      "s_waitcnt lgkmcnt(4)\n"
      "v_mfma_f32_32x32x16_bf16 v[112:127], v[168:171], v[48:51], v[112:127]\n"
      "v_lshlrev_b32 %[t0], 16, v50\n"
      "v_and_b32 %[t1], %[msk], v50\n"
      "v_add_f32 %[c2], %[c2], %[t0]\n"
      "v_add_f32 %[c2], %[c2], %[t1]\n"
      "v_mfma_f32_32x32x16_bf16 v[80:95], v[52:55], v[172:175], v[80:95]\n"
      "v_lshlrev_b32 %[t0], 16, v55\n"
      "v_and_b32 %[t1], %[msk], v55\n"
      "v_add_f32 %[c0], %[c0], %[t0]\n"
      "v_add_f32 %[c0], %[c0], %[t1]\n"
      "s_waitcnt lgkmcnt(0)\n"
      "v_mfma_f32_32x32x16_bf16 v[96:111], v[56:59], v[172:175], v[96:111]\n"
      "v_lshlrev_b32 %[t0], 16, v59\n"
      "v_and_b32 %[t1], %[msk], v59\n"
      "v_add_f32 %[c1], %[c1], %[t0]\n"
      "v_add_f32 %[c1], %[c1], %[t1]\n"
      "v_mfma_f32_32x32x16_bf16 v[112:127], v[172:175], v[60:63], v[112:127]\n"
      "v_lshlrev_b32 %[t0], 16, v63\n"
      "v_and_b32 %[t1], %[msk], v63\n"
      "v_add_f32 %[c2], %[c2], %[t0]\n"
      "v_add_f32 %[c2], %[c2], %[t1]\n"
      "s_nop 15\n"
      "s_branch 3f\n"
      "2:\n"
      "ds_read_b64_tr_b16 v[48:49], %[b0] offset:0\n"
      "ds_read_b64_tr_b16 v[50:51], %[b0] offset:768\n"
      "ds_read_b64_tr_b16 v[52:53], %[b0] offset:64\n"
      "ds_read_b64_tr_b16 v[54:55], %[b0] offset:832\n"
      "ds_read_b64_tr_b16 v[56:57], %[b0] offset:128\n"
      "ds_read_b64_tr_b16 v[58:59], %[b0] offset:896\n"
      "ds_read_b64_tr_b16 v[60:61], %[b1] offset:0\n"
      "ds_read_b64_tr_b16 v[62:63], %[b1] offset:768\n"
      "ds_read_b64_tr_b16 v[64:65], %[b1] offset:64\n"
      "ds_read_b64_tr_b16 v[66:67], %[b1] offset:832\n"
      "ds_read_b64_tr_b16 v[68:69], %[b1] offset:128\n"
      "ds_read_b64_tr_b16 v[70:71], %[b1] offset:896\n"
      "ds_read_b64_tr_b16 v[72:73], %[b2] offset:0\n"
      "ds_read_b64_tr_b16 v[74:75], %[b2] offset:768\n"
      "s_waitcnt lgkmcnt(10)\n"
      "v_mfma_f32_32x32x16_bf16 v[80:95], v[128:131], v[48:51], 0\n"
      "ds_read_b64_tr_b16 v[76:77], %[b2] offset:64\n"
      "ds_read_b64_tr_b16 v[78:79], %[b2] offset:832\n"
      "v_lshlrev_b32 %[t0], 16, v48\n"
      "v_and_b32 %[t1], %[msk], v48\n"
      "v_add_f32 %[c0], %[c0], %[t0]\n"
      "v_add_f32 %[c0], %[c0], %[t1]\n"
      "v_mfma_f32_32x32x16_bf16 v[96:111], v[128:131], v[52:55], 0\n"
      "ds_read_b64_tr_b16 v[48:49], %[b2] offset:128\n"
      "ds_read_b64_tr_b16 v[50:51], %[b2] offset:896\n"
      "v_lshlrev_b32 %[t0], 16, v52\n"
      "v_and_b32 %[t1], %[msk], v52\n"
      "v_add_f32 %[c1], %[c1], %[t0]\n"
      "v_add_f32 %[c1], %[c1], %[t1]\n"
      "s_waitcnt lgkmcnt(10)\n"
      "v_mfma_f32_32x32x16_bf16 v[112:127], v[128:131], v[56:59], 0\n"
      "ds_read_b64_tr_b16 v[52:53], %[b3] offset:0\n"
      "ds_read_b64_tr_b16 v[54:55], %[b3] offset:768\n"
      "v_lshlrev_b32 %[t0], 16, v56\n"
      "v_and_b32 %[t1], %[msk], v56\n"
      "v_add_f32 %[c2], %[c2], %[t0]\n"
      "v_add_f32 %[c2], %[c2], %[t1]\n"
      "v_mfma_f32_32x32x16_bf16 v[80:95], v[132:135], v[60:63], v[80:95]\n"
      "ds_read_b64_tr_b16 v[56:57], %[b3] offset:64\n"
      "ds_read_b64_tr_b16 v[58:59], %[b3] offset:832\n"
      "v_lshlrev_b32 %[t0], 16, v61\n"
      "v_and_b32 %[t1], %[msk], v61\n"
      "v_add_f32 %[c0], %[c0], %[t0]\n"
      "v_add_f32 %[c0], %[c0], %[t1]\n"
      "s_waitcnt lgkmcnt(10)\n"
      "v_mfma_f32_32x32x16_bf16 v[96:111], v[132:135], v[64:67], v[96:111]\n"
      "ds_read_b64_tr_b16 v[60:61], %[b3] offset:128\n"
      "ds_read_b64_tr_b16 v[62:63], %[b3] offset:896\n"
      "v_lshlrev_b32 %[t0], 16, v65\n"
      "v_and_b32 %[t1], %[msk], v65\n"
      "v_add_f32 %[c1], %[c1], %[t0]\n"
      "v_add_f32 %[c1], %[c1], %[t1]\n"
      "v_mfma_f32_32x32x16_bf16 v[112:127], v[132:135], v[68:71], v[112:127]\n"
      "ds_read_b64_tr_b16 v[64:65], %[b0] offset:12288\n"
      "ds_read_b64_tr_b16 v[66:67], %[b0] offset:13056\n"
      "v_lshlrev_b32 %[t0], 16, v69\n"
      "v_and_b32 %[t1], %[msk], v69\n"
      "v_add_f32 %[c2], %[c2], %[t0]\n"
      "v_add_f32 %[c2], %[c2], %[t1]\n"
      "s_waitcnt lgkmcnt(10)\n"
      "v_mfma_f32_32x32x16_bf16 v[80:95], v[136:139], v[72:75], v[80:95]\n"
      "ds_read_b64_tr_b16 v[68:69], %[b0] offset:12352\n"
      "ds_read_b64_tr_b16 v[70:71], %[b0] offset:13120\n"
      "v_lshlrev_b32 %[t0], 16, v74\n"
      "v_and_b32 %[t1], %[msk], v74\n"
      "v_add_f32 %[c0], %[c0], %[t0]\n"
      "v_add_f32 %[c0], %[c0], %[t1]\n"
      "v_mfma_f32_32x32x16_bf16 v[96:111], v[136:139], v[76:79], v[96:111]\n"
      "ds_read_b64_tr_b16 v[72:73], %[b0] offset:12416\n"
      "ds_read_b64_tr_b16 v[74:75], %[b0] offset:13184\n"
      "v_lshlrev_b32 %[t0], 16, v78\n"
      "v_and_b32 %[t1], %[msk], v78\n"
      "v_add_f32 %[c1], %[c1], %[t0]\n"
      "v_add_f32 %[c1], %[c1], %[t1]\n"
      "s_waitcnt lgkmcnt(10)\n"
      "v_mfma_f32_32x32x16_bf16 v[112:127], v[136:139], v[48:51], v[112:127]\n"
      "ds_read_b64_tr_b16 v[76:77], %[b1] offset:12288\n"
      "ds_read_b64_tr_b16 v[78:79], %[b1] offset:13056\n"
      "v_lshlrev_b32 %[t0], 16, v50\n"
      "v_and_b32 %[t1], %[msk], v50\n"
      "v_add_f32 %[c2], %[c2], %[t0]\n"
      "v_add_f32 %[c2], %[c2], %[t1]\n"
      "v_mfma_f32_32x32x16_bf16 v[80:95], v[140:143], v[52:55], v[80:95]\n"
      "ds_read_b64_tr_b16 v[48:49], %[b1] offset:12352\n"
      "ds_read_b64_tr_b16 v[50:51], %[b1] offset:13120\n"
      "v_lshlrev_b32 %[t0], 16, v55\n"
      "v_and_b32 %[t1], %[msk], v55\n"
      "v_add_f32 %[c0], %[c0], %[t0]\n"
      "v_add_f32 %[c0], %[c0], %[t1]\n"
      "s_waitcnt lgkmcnt(10)\n"
      "v_mfma_f32_32x32x16_bf16 v[96:111], v[140:143], v[56:59], v[96:111]\n"
      "ds_read_b64_tr_b16 v[52:53], %[b1] offset:12416\n"
      "ds_read_b64_tr_b16 v[54:55], %[b1] offset:13184\n"
      "v_lshlrev_b32 %[t0], 16, v59\n"
      "v_and_b32 %[t1], %[msk], v59\n"
      "v_add_f32 %[c1], %[c1], %[t0]\n"
      "v_add_f32 %[c1], %[c1], %[t1]\n"
      "v_mfma_f32_32x32x16_bf16 v[112:127], v[140:143], v[60:63], v[112:127]\n"
      "ds_read_b64_tr_b16 v[56:57], %[b2] offset:12288\n"
      "ds_read_b64_tr_b16 v[58:59], %[b2] offset:13056\n"
      "v_lshlrev_b32 %[t0], 16, v63\n"
      "v_and_b32 %[t1], %[msk], v63\n"
      "v_add_f32 %[c2], %[c2], %[t0]\n"
      "v_add_f32 %[c2], %[c2], %[t1]\n"
      "s_waitcnt lgkmcnt(10)\n"
      "v_mfma_f32_32x32x16_bf16 v[80:95], v[144:147], v[64:67], v[80:95]\n"
      "ds_read_b64_tr_b16 v[60:61], %[b2] offset:12352\n"
      "ds_read_b64_tr_b16 v[62:63], %[b2] offset:13120\n"
      "v_lshlrev_b32 %[t0], 16, v64\n"
      "v_and_b32 %[t1], %[msk], v64\n"
      "v_add_f32 %[c0], %[c0], %[t0]\n"
      "v_add_f32 %[c0], %[c0], %[t1]\n"
      "v_mfma_f32_32x32x16_bf16 v[96:111], v[144:147], v[68:71], v[96:111]\n"
      "ds_read_b64_tr_b16 v[64:65], %[b2] offset:12416\n"
      "ds_read_b64_tr_b16 v[66:67], %[b2] offset:13184\n"
      "v_lshlrev_b32 %[t0], 16, v68\n"
      "v_and_b32 %[t1], %[msk], v68\n"
      "v_add_f32 %[c1], %[c1], %[t0]\n"
      "v_add_f32 %[c1], %[c1], %[t1]\n"
      "s_waitcnt lgkmcnt(10)\n"
      "v_mfma_f32_32x32x16_bf16 v[112:127], v[144:147], v[72:75], v[112:127]\n"
      "ds_read_b64_tr_b16 v[68:69], %[b3] offset:12288\n"
      "ds_read_b64_tr_b16 v[70:71], %[b3] offset:13056\n"
      "v_lshlrev_b32 %[t0], 16, v72\n"
      "v_and_b32 %[t1], %[msk], v72\n"
      "v_add_f32 %[c2], %[c2], %[t0]\n"
      "v_add_f32 %[c2], %[c2], %[t1]\n"
      "v_mfma_f32_32x32x16_bf16 v[80:95], v[148:151], v[76:79], v[80:95]\n"
      "ds_read_b64_tr_b16 v[72:73], %[b3] offset:12352\n"
      "ds_read_b64_tr_b16 v[74:75], %[b3] offset:13120\n"
      "v_lshlrev_b32 %[t0], 16, v77\n"
      "v_and_b32 %[t1], %[msk], v77\n"
      "v_add_f32 %[c0], %[c0], %[t0]\n"
      "v_add_f32 %[c0], %[c0], %[t1]\n"
      "s_waitcnt lgkmcnt(10)\n"
      "v_mfma_f32_32x32x16_bf16 v[96:111], v[148:151], v[48:51], v[96:111]\n"
      "ds_read_b64_tr_b16 v[76:77], %[b3] offset:12416\n"
      "ds_read_b64_tr_b16 v[78:79], %[b3] offset:13184\n"
      "v_lshlrev_b32 %[t0], 16, v49\n"
      "v_and_b32 %[t1], %[msk], v49\n"
      "v_add_f32 %[c1], %[c1], %[t0]\n"
      "v_add_f32 %[c1], %[c1], %[t1]\n"
      "v_mfma_f32_32x32x16_bf16 v[112:127], v[148:151], v[52:55], v[112:127]\n"
      "ds_read_b64_tr_b16 v[48:49], %[b0] offset:24576\n"
      "ds_read_b64_tr_b16 v[50:51], %[b0] offset:25344\n"
      "v_lshlrev_b32 %[t0], 16, v53\n"
      "v_and_b32 %[t1], %[msk], v53\n"
      "v_add_f32 %[c2], %[c2], %[t0]\n"
      "v_add_f32 %[c2], %[c2], %[t1]\n"
      "s_waitcnt lgkmcnt(10)\n"
      "v_mfma_f32_32x32x16_bf16 v[80:95], v[152:155], v[56:59], v[80:95]\n"
      "ds_read_b64_tr_b16 v[52:53], %[b0] offset:24640\n"
      "ds_read_b64_tr_b16 v[54:55], %[b0] offset:25408\n"
      "v_lshlrev_b32 %[t0], 16, v58\n"
      "v_and_b32 %[t1], %[msk], v58\n"
      "v_add_f32 %[c0], %[c0], %[t0]\n"
      "v_add_f32 %[c0], %[c0], %[t1]\n"
      "v_mfma_f32_32x32x16_bf16 v[96:111], v[152:155], v[60:63], v[96:111]\n"
      "ds_read_b64_tr_b16 v[56:57], %[b0] offset:24704\n"
      "ds_read_b64_tr_b16 v[58:59], %[b0] offset:25472\n"
      "v_lshlrev_b32 %[t0], 16, v62\n"
      "v_and_b32 %[t1], %[msk], v62\n"
      "v_add_f32 %[c1], %[c1], %[t0]\n"
      "v_add_f32 %[c1], %[c1], %[t1]\n"
      "s_waitcnt lgkmcnt(10)\n"
      "v_mfma_f32_32x32x16_bf16 v[112:127], v[152:155], v[64:67], v[112:127]\n"
      "ds_read_b64_tr_b16 v[60:61], %[b1] offset:24576\n"
      "ds_read_b64_tr_b16 v[62:63], %[b1] offset:25344\n"
      "v_lshlrev_b32 %[t0], 16, v66\n"
      "v_and_b32 %[t1], %[msk], v66\n"
      "v_add_f32 %[c2], %[c2], %[t0]\n"
      "v_add_f32 %[c2], %[c2], %[t1]\n"
      "v_mfma_f32_32x32x16_bf16 v[80:95], v[156:159], v[68:71], v[80:95]\n"
      "ds_read_b64_tr_b16 v[64:65], %[b1] offset:24640\n"
      "ds_read_b64_tr_b16 v[66:67], %[b1] offset:25408\n"
      "v_lshlrev_b32 %[t0], 16, v71\n"
      "v_and_b32 %[t1], %[msk], v71\n"
      "v_add_f32 %[c0], %[c0], %[t0]\n"
      "v_add_f32 %[c0], %[c0], %[t1]\n"
      "s_waitcnt lgkmcnt(10)\n"
      "v_mfma_f32_32x32x16_bf16 v[96:111], v[156:159], v[72:75], v[96:111]\n"
      "ds_read_b64_tr_b16 v[68:69], %[b1] offset:24704\n"
      "ds_read_b64_tr_b16 v[70:71], %[b1] offset:25472\n"
      "v_lshlrev_b32 %[t0], 16, v75\n"
      "v_and_b32 %[t1], %[msk], v75\n"
      "v_add_f32 %[c1], %[c1], %[t0]\n"
      "v_add_f32 %[c1], %[c1], %[t1]\n"
      "v_mfma_f32_32x32x16_bf16 v[112:127], v[156:159], v[76:79], v[112:127]\n"
      "ds_read_b64_tr_b16 v[72:73], %[b2] offset:24576\n"
      "ds_read_b64_tr_b16 v[74:75], %[b2] offset:25344\n"
      "v_lshlrev_b32 %[t0], 16, v79\n"
      "v_and_b32 %[t1], %[msk], v79\n"
      "v_add_f32 %[c2], %[c2], %[t0]\n"
      "v_add_f32 %[c2], %[c2], %[t1]\n"
      "s_waitcnt lgkmcnt(10)\n"
      "v_mfma_f32_32x32x16_bf16 v[80:95], v[160:163], v[48:51], v[80:95]\n"
      "ds_read_b64_tr_b16 v[76:77], %[b2] offset:24640\n"
      "ds_read_b64_tr_b16 v[78:79], %[b2] offset:25408\n"
      "v_lshlrev_b32 %[t0], 16, v48\n"
      "v_and_b32 %[t1], %[msk], v48\n"
      "v_add_f32 %[c0], %[c0], %[t0]\n"
      "v_add_f32 %[c0], %[c0], %[t1]\n"
      "v_mfma_f32_32x32x16_bf16 v[96:111], v[160:163], v[52:55], v[96:111]\n"
      "ds_read_b64_tr_b16 v[48:49], %[b2] offset:24704\n"
      "ds_read_b64_tr_b16 v[50:51], %[b2] offset:25472\n"
      "v_lshlrev_b32 %[t0], 16, v52\n"
      "v_and_b32 %[t1], %[msk], v52\n"
      "v_add_f32 %[c1], %[c1], %[t0]\n"
      "v_add_f32 %[c1], %[c1], %[t1]\n"
      "s_waitcnt lgkmcnt(10)\n"
      "v_mfma_f32_32x32x16_bf16 v[112:127], v[160:163], v[56:59], v[112:127]\n"
      "ds_read_b64_tr_b16 v[52:53], %[b3] offset:24576\n"
      "ds_read_b64_tr_b16 v[54:55], %[b3] offset:25344\n"
      "v_lshlrev_b32 %[t0], 16, v56\n"
      "v_and_b32 %[t1], %[msk], v56\n"
      "v_add_f32 %[c2], %[c2], %[t0]\n"
      "v_add_f32 %[c2], %[c2], %[t1]\n"
      "v_mfma_f32_32x32x16_bf16 v[80:95], v[164:167], v[60:63], v[80:95]\n"
      "ds_read_b64_tr_b16 v[56:57], %[b3] offset:24640\n"
      "ds_read_b64_tr_b16 v[58:59], %[b3] offset:25408\n"
      "v_lshlrev_b32 %[t0], 16, v61\n"
      "v_and_b32 %[t1], %[msk], v61\n"
      "v_add_f32 %[c0], %[c0], %[t0]\n"
      "v_add_f32 %[c0], %[c0], %[t1]\n"
      "s_waitcnt lgkmcnt(10)\n"
      "v_mfma_f32_32x32x16_bf16 v[96:111], v[164:167], v[64:67], v[96:111]\n"
      "ds_read_b64_tr_b16 v[60:61], %[b3] offset:24704\n"
      "ds_read_b64_tr_b16 v[62:63], %[b3] offset:25472\n"
      "v_lshlrev_b32 %[t0], 16, v65\n"
      "v_and_b32 %[t1], %[msk], v65\n"
      "v_add_f32 %[c1], %[c1], %[t0]\n"
      "v_add_f32 %[c1], %[c1], %[t1]\n"
      "v_mfma_f32_32x32x16_bf16 v[112:127], v[164:167], v[68:71], v[112:127]\n"
      "v_lshlrev_b32 %[t0], 16, v69\n"
      "v_and_b32 %[t1], %[msk], v69\n"
      "v_add_f32 %[c2], %[c2], %[t0]\n"
      "v_add_f32 %[c2], %[c2], %[t1]\n"
      "s_waitcnt lgkmcnt(8)\n"
      "v_mfma_f32_32x32x16_bf16 v[80:95], v[168:171], v[72:75], v[80:95]\n"
      "v_lshlrev_b32 %[t0], 16, v74\n"
      "v_and_b32 %[t1], %[msk], v74\n"
      "v_add_f32 %[c0], %[c0], %[t0]\n"
      "v_add_f32 %[c0], %[c0], %[t1]\n"
      "v_mfma_f32_32x32x16_bf16 v[96:111], v[168:171], v[76:79], v[96:111]\n"
      "v_lshlrev_b32 %[t0], 16, v78\n"
      "v_and_b32 %[t1], %[msk], v78\n"
      "v_add_f32 %[c1], %[c1], %[t0]\n"
      "v_add_f32 %[c1], %[c1], %[t1]\n"
      "s_waitcnt lgkmcnt(4)\n"
      "v_mfma_f32_32x32x16_bf16 v[112:127], v[168:171], v[48:51], v[112:127]\n"
      "v_lshlrev_b32 %[t0], 16, v50\n"
      "v_and_b32 %[t1], %[msk], v50\n"
      "v_add_f32 %[c2], %[c2], %[t0]\n"
      "v_add_f32 %[c2], %[c2], %[t1]\n"
      "v_mfma_f32_32x32x16_bf16 v[80:95], v[172:175], v[52:55], v[80:95]\n"
      "v_lshlrev_b32 %[t0], 16, v55\n"
      "v_and_b32 %[t1], %[msk], v55\n"
      "v_add_f32 %[c0], %[c0], %[t0]\n"
      "v_add_f32 %[c0], %[c0], %[t1]\n"
      "s_waitcnt lgkmcnt(0)\n"
      "v_mfma_f32_32x32x16_bf16 v[96:111], v[172:175], v[56:59], v[96:111]\n"
      "v_lshlrev_b32 %[t0], 16, v59\n"
      "v_and_b32 %[t1], %[msk], v59\n"
      "v_add_f32 %[c1], %[c1], %[t0]\n"
      "v_add_f32 %[c1], %[c1], %[t1]\n"
      "v_mfma_f32_32x32x16_bf16 v[112:127], v[172:175], v[60:63], v[112:127]\n"
      "v_lshlrev_b32 %[t0], 16, v63\n"
      "v_and_b32 %[t1], %[msk], v63\n"
      "v_add_f32 %[c2], %[c2], %[t0]\n"
      "v_add_f32 %[c2], %[c2], %[t1]\n"
      "s_nop 15\n"
      "3:\n"
        : "={v[80:95]}"(a0), "={v[96:111]}"(a1), "={v[112:127]}"(a2), CSUM : XIN_LO, B4, [msk] "s"(msk), [cg] "s"(cg) : QCLOB, "scc", "memory");
    BAR();
    asm volatile(
      "s_cmp_gt_u32 %[cg], 10\n"
      "s_cbranch_scc1 2f\n"
      "s_cmp_eq_u32 %[cg], 10\n"
      "s_cbranch_scc1 1f\n"
      "ds_read_b64_tr_b16 v[48:49], %[b0] offset:36864\n"
      "ds_read_b64_tr_b16 v[50:51], %[b0] offset:37632\n"
      "ds_read_b64_tr_b16 v[52:53], %[b0] offset:36928\n"
      "ds_read_b64_tr_b16 v[54:55], %[b0] offset:37696\n"
      "ds_read_b64_tr_b16 v[56:57], %[b0] offset:36992\n"
      "ds_read_b64_tr_b16 v[58:59], %[b0] offset:37760\n"
      "ds_read_b64_tr_b16 v[60:61], %[b1] offset:36864\n"
      "ds_read_b64_tr_b16 v[62:63], %[b1] offset:37632\n"
      "ds_read_b64_tr_b16 v[64:65], %[b1] offset:36928\n"
      "ds_read_b64_tr_b16 v[66:67], %[b1] offset:37696\n"
      "ds_read_b64_tr_b16 v[68:69], %[b1] offset:36992\n"
      "ds_read_b64_tr_b16 v[70:71], %[b1] offset:37760\n"
      "ds_read_b64_tr_b16 v[72:73], %[b2] offset:36864\n"
      "ds_read_b64_tr_b16 v[74:75], %[b2] offset:37632\n"
      "s_waitcnt lgkmcnt(10)\n"
      "v_mfma_f32_32x32x16_bf16 v[80:95], v[48:51], v[176:179], v[80:95]\n"
      "ds_read_b64_tr_b16 v[76:77], %[b2] offset:36928\n"
      "ds_read_b64_tr_b16 v[78:79], %[b2] offset:37696\n"
      "v_lshlrev_b32 %[t0], 16, v48\n"
      "v_and_b32 %[t1], %[msk], v48\n"
      "v_add_f32 %[c0], %[c0], %[t0]\n"
      "ds_read_b128 v[192:195], %[a0] offset:0\n"
      "ds_read_b128 v[196:199], %[a1] offset:0\n"
      "v_add_f32 %[c0], %[c0], %[t1]\n"
      "v_mfma_f32_32x32x16_bf16 v[96:111], v[52:55], v[176:179], v[96:111]\n"
      "ds_read_b64_tr_b16 v[48:49], %[b2] offset:36992\n"
      "ds_read_b64_tr_b16 v[50:51], %[b2] offset:37760\n"
      "v_lshlrev_b32 %[t0], 16, v52\n"
      "v_and_b32 %[t1], %[msk], v52\n"
      "v_add_f32 %[c1], %[c1], %[t0]\n"
      "ds_read_b128 v[200:203], %[a2] offset:0\n"
      "ds_read_b128 v[204:207], %[a3] offset:0\n"
      "v_add_f32 %[c1], %[c1], %[t1]\n"
      "s_waitcnt lgkmcnt(14)\n"
      "v_mfma_f32_32x32x16_bf16 v[112:127], v[56:59], v[176:179], v[112:127]\n"
      "ds_read_b64_tr_b16 v[52:53], %[b3] offset:36864\n"
      "ds_read_b64_tr_b16 v[54:55], %[b3] offset:37632\n"
      "v_lshlrev_b32 %[t0], 16, v56\n"
      "v_and_b32 %[t1], %[msk], v56\n"
      "v_add_f32 %[c2], %[c2], %[t0]\n"
      "ds_read_b128 v[208:211], %[a4] offset:0\n"
      "v_add_f32 %[c2], %[c2], %[t1]\n"
      "v_mfma_f32_32x32x16_bf16 v[80:95], v[60:63], v[180:183], v[80:95]\n"
      "ds_read_b64_tr_b16 v[56:57], %[b3] offset:36928\n"
      "ds_read_b64_tr_b16 v[58:59], %[b3] offset:37696\n"
      "v_lshlrev_b32 %[t0], 16, v61\n"
      "v_and_b32 %[t1], %[msk], v61\n"
      "v_add_f32 %[c0], %[c0], %[t0]\n"
      "ds_read_b128 v[212:215], %[a5] offset:0\n"
      "ds_read_b128 v[216:219], %[a6] offset:0\n"
      "v_add_f32 %[c0], %[c0], %[t1]\n"
      "s_waitcnt lgkmcnt(15)\n"
      "v_mfma_f32_32x32x16_bf16 v[96:111], v[64:67], v[180:183], v[96:111]\n"
      "ds_read_b64_tr_b16 v[60:61], %[b3] offset:36992\n"
      "ds_read_b64_tr_b16 v[62:63], %[b3] offset:37760\n"
      "v_lshlrev_b32 %[t0], 16, v65\n"
      "v_and_b32 %[t1], %[msk], v65\n"
      "v_add_f32 %[c1], %[c1], %[t0]\n"
      "ds_read_b128 v[220:223], %[a7] offset:0\n"
      "v_add_f32 %[c1], %[c1], %[t1]\n"
      "v_mfma_f32_32x32x16_bf16 v[112:127], v[68:71], v[180:183], v[112:127]\n"
      "v_lshlrev_b32 %[t0], 16, v69\n"
      "v_and_b32 %[t1], %[msk], v69\n"
      "v_add_f32 %[c2], %[c2], %[t0]\n"
      "ds_read_b128 v[224:227], %[a0] offset:256\n"
      "ds_read_b128 v[228:231], %[a1] offset:256\n"
      "v_add_f32 %[c2], %[c2], %[t1]\n"
      "s_waitcnt lgkmcnt(15)\n"
      "v_mfma_f32_32x32x16_bf16 v[80:95], v[72:75], v[184:187], v[80:95]\n"
      "v_lshlrev_b32 %[t0], 16, v74\n"
      "v_and_b32 %[t1], %[msk], v74\n"
      "v_add_f32 %[c0], %[c0], %[t0]\n"
      "ds_read_b128 v[232:235], %[a2] offset:256\n"
      "ds_read_b128 v[236:239], %[a3] offset:256\n"
      "v_add_f32 %[c0], %[c0], %[t1]\n"
      "v_mfma_f32_32x32x16_bf16 v[96:111], v[76:79], v[184:187], v[96:111]\n"
      "v_lshlrev_b32 %[t0], 16, v78\n"
      "v_and_b32 %[t1], %[msk], v78\n"
      "v_add_f32 %[c1], %[c1], %[t0]\n"
      "ds_read_b128 v[240:243], %[a4] offset:256\n"
      "v_add_f32 %[c1], %[c1], %[t1]\n"
      "s_waitcnt lgkmcnt(13)\n"
      "v_mfma_f32_32x32x16_bf16 v[112:127], v[48:51], v[184:187], v[112:127]\n"
      "v_lshlrev_b32 %[t0], 16, v50\n"
      "v_and_b32 %[t1], %[msk], v50\n"
      "v_add_f32 %[c2], %[c2], %[t0]\n"
      "ds_read_b128 v[244:247], %[a5] offset:256\n"
      "ds_read_b128 v[248:251], %[a6] offset:256\n"
      "v_add_f32 %[c2], %[c2], %[t1]\n"
      "v_mfma_f32_32x32x16_bf16 v[80:95], v[52:55], v[188:191], v[80:95]\n"
      "v_lshlrev_b32 %[t0], 16, v55\n"
      "v_and_b32 %[t1], %[msk], v55\n"
      "v_add_f32 %[c0], %[c0], %[t0]\n"
      "ds_read_b128 v[252:255], %[a7] offset:256\n"
      "v_add_f32 %[c0], %[c0], %[t1]\n"
      "s_waitcnt lgkmcnt(9)\n"
      "v_mfma_f32_32x32x16_bf16 v[96:111], v[56:59], v[188:191], v[96:111]\n"
      "v_lshlrev_b32 %[t0], 16, v59\n"
      "v_and_b32 %[t1], %[msk], v59\n"
      "v_add_f32 %[c1], %[c1], %[t0]\n"
      "v_add_f32 %[c1], %[c1], %[t1]\n"
      "v_mfma_f32_32x32x16_bf16 v[112:127], v[60:63], v[188:191], v[112:127]\n"
      "v_lshlrev_b32 %[t0], 16, v63\n"
      "v_and_b32 %[t1], %[msk], v63\n"
      "v_add_f32 %[c2], %[c2], %[t0]\n"
      "v_add_f32 %[c2], %[c2], %[t1]\n"
      "s_waitcnt lgkmcnt(0)\n"
      "s_nop 15\n"
      "s_branch 3f\n"
      "1:\n"
      "ds_read_b64_tr_b16 v[48:49], %[b0] offset:36864\n"
      "ds_read_b64_tr_b16 v[50:51], %[b0] offset:37632\n"
      "ds_read_b64_tr_b16 v[52:53], %[b0] offset:36928\n"
      "ds_read_b64_tr_b16 v[54:55], %[b0] offset:37696\n"
      "ds_read_b64_tr_b16 v[56:57], %[b0] offset:36992\n"
      "ds_read_b64_tr_b16 v[58:59], %[b0] offset:37760\n"
      "ds_read_b64_tr_b16 v[60:61], %[b1] offset:36864\n"
      "ds_read_b64_tr_b16 v[62:63], %[b1] offset:37632\n"
      "ds_read_b64_tr_b16 v[64:65], %[b1] offset:36928\n"
      "ds_read_b64_tr_b16 v[66:67], %[b1] offset:37696\n"
      "ds_read_b64_tr_b16 v[68:69], %[b1] offset:36992\n"
      "ds_read_b64_tr_b16 v[70:71], %[b1] offset:37760\n"
      "ds_read_b64_tr_b16 v[72:73], %[b2] offset:36864\n"
      "ds_read_b64_tr_b16 v[74:75], %[b2] offset:37632\n"
      "s_waitcnt lgkmcnt(10)\n"
      "v_mfma_f32_32x32x16_bf16 v[80:95], v[48:51], v[176:179], v[80:95]\n"
      "ds_read_b64_tr_b16 v[76:77], %[b2] offset:36928\n"
      "ds_read_b64_tr_b16 v[78:79], %[b2] offset:37696\n"
      "v_lshlrev_b32 %[t0], 16, v48\n"
      "v_and_b32 %[t1], %[msk], v48\n"
      "v_add_f32 %[c0], %[c0], %[t0]\n"
      "ds_read_b128 v[192:195], %[a0] offset:0\n"
      "ds_read_b128 v[196:199], %[a1] offset:0\n"
      "v_add_f32 %[c0], %[c0], %[t1]\n"
      "v_mfma_f32_32x32x16_bf16 v[96:111], v[52:55], v[176:179], v[96:111]\n"
      "ds_read_b64_tr_b16 v[48:49], %[b2] offset:36992\n"
      "ds_read_b64_tr_b16 v[50:51], %[b2] offset:37760\n"
      "v_lshlrev_b32 %[t0], 16, v52\n"
      "v_and_b32 %[t1], %[msk], v52\n"
      "v_add_f32 %[c1], %[c1], %[t0]\n"
      "ds_read_b128 v[200:203], %[a2] offset:0\n"
      "ds_read_b128 v[204:207], %[a3] offset:0\n"
      "v_add_f32 %[c1], %[c1], %[t1]\n"
      "s_waitcnt lgkmcnt(14)\n"
      "v_mfma_f32_32x32x16_bf16 v[112:127], v[176:179], v[56:59], v[112:127]\n"
      "ds_read_b64_tr_b16 v[52:53], %[b3] offset:36864\n"
      "ds_read_b64_tr_b16 v[54:55], %[b3] offset:37632\n"
      "v_lshlrev_b32 %[t0], 16, v56\n"
      "v_and_b32 %[t1], %[msk], v56\n"
      "v_add_f32 %[c2], %[c2], %[t0]\n"
      "ds_read_b128 v[208:211], %[a4] offset:0\n"
      "v_add_f32 %[c2], %[c2], %[t1]\n"
      "v_mfma_f32_32x32x16_bf16 v[80:95], v[60:63], v[180:183], v[80:95]\n"
      "ds_read_b64_tr_b16 v[56:57], %[b3] offset:36928\n"
      "ds_read_b64_tr_b16 v[58:59], %[b3] offset:37696\n"
      "v_lshlrev_b32 %[t0], 16, v61\n"
      "v_and_b32 %[t1], %[msk], v61\n"
      "v_add_f32 %[c0], %[c0], %[t0]\n"
      "ds_read_b128 v[212:215], %[a5] offset:0\n"
      "ds_read_b128 v[216:219], %[a6] offset:0\n"
      "v_add_f32 %[c0], %[c0], %[t1]\n"
      "s_waitcnt lgkmcnt(15)\n"
      "v_mfma_f32_32x32x16_bf16 v[96:111], v[64:67], v[180:183], v[96:111]\n"
      "ds_read_b64_tr_b16 v[60:61], %[b3] offset:36992\n"
      "ds_read_b64_tr_b16 v[62:63], %[b3] offset:37760\n"
      "v_lshlrev_b32 %[t0], 16, v65\n"
      "v_and_b32 %[t1], %[msk], v65\n"
      "v_add_f32 %[c1], %[c1], %[t0]\n"
      "ds_read_b128 v[220:223], %[a7] offset:0\n"
      "v_add_f32 %[c1], %[c1], %[t1]\n"
      "v_mfma_f32_32x32x16_bf16 v[112:127], v[180:183], v[68:71], v[112:127]\n"
      "v_lshlrev_b32 %[t0], 16, v69\n"
      "v_and_b32 %[t1], %[msk], v69\n"
      "v_add_f32 %[c2], %[c2], %[t0]\n"
      "ds_read_b128 v[224:227], %[a0] offset:256\n"
      "ds_read_b128 v[228:231], %[a1] offset:256\n"
      "v_add_f32 %[c2], %[c2], %[t1]\n"
      "s_waitcnt lgkmcnt(15)\n"
      "v_mfma_f32_32x32x16_bf16 v[80:95], v[72:75], v[184:187], v[80:95]\n"
      "v_lshlrev_b32 %[t0], 16, v74\n"
      "v_and_b32 %[t1], %[msk], v74\n"
      "v_add_f32 %[c0], %[c0], %[t0]\n"
      "ds_read_b128 v[232:235], %[a2] offset:256\n"
      "ds_read_b128 v[236:239], %[a3] offset:256\n"
      "v_add_f32 %[c0], %[c0], %[t1]\n"
      "v_mfma_f32_32x32x16_bf16 v[96:111], v[76:79], v[184:187], v[96:111]\n"
      "v_lshlrev_b32 %[t0], 16, v78\n"
      "v_and_b32 %[t1], %[msk], v78\n"
      "v_add_f32 %[c1], %[c1], %[t0]\n"
      "ds_read_b128 v[240:243], %[a4] offset:256\n"
      "v_add_f32 %[c1], %[c1], %[t1]\n"
      "s_waitcnt lgkmcnt(13)\n"
      "v_mfma_f32_32x32x16_bf16 v[112:127], v[184:187], v[48:51], v[112:127]\n"
      "v_lshlrev_b32 %[t0], 16, v50\n"
      "v_and_b32 %[t1], %[msk], v50\n"
      "v_add_f32 %[c2], %[c2], %[t0]\n"
      "ds_read_b128 v[244:247], %[a5] offset:256\n"
      "ds_read_b128 v[248:251], %[a6] offset:256\n"
      "v_add_f32 %[c2], %[c2], %[t1]\n"
      "v_mfma_f32_32x32x16_bf16 v[80:95], v[52:55], v[188:191], v[80:95]\n"
      "v_lshlrev_b32 %[t0], 16, v55\n"
      "v_and_b32 %[t1], %[msk], v55\n"
      "v_add_f32 %[c0], %[c0], %[t0]\n"
      "ds_read_b128 v[252:255], %[a7] offset:256\n"
      "v_add_f32 %[c0], %[c0], %[t1]\n"
      "s_waitcnt lgkmcnt(9)\n"
      "v_mfma_f32_32x32x16_bf16 v[96:111], v[56:59], v[188:191], v[96:111]\n"
      "v_lshlrev_b32 %[t0], 16, v59\n"
      "v_and_b32 %[t1], %[msk], v59\n"
      "v_add_f32 %[c1], %[c1], %[t0]\n"
      "v_add_f32 %[c1], %[c1], %[t1]\n"
      "v_mfma_f32_32x32x16_bf16 v[112:127], v[188:191], v[60:63], v[112:127]\n"
      "v_lshlrev_b32 %[t0], 16, v63\n"
      "v_and_b32 %[t1], %[msk], v63\n"
      "v_add_f32 %[c2], %[c2], %[t0]\n"
      "v_add_f32 %[c2], %[c2], %[t1]\n"
      "s_waitcnt lgkmcnt(0)\n"
      "s_nop 15\n"
      "s_branch 3f\n"
      "2:\n"
      "ds_read_b64_tr_b16 v[48:49], %[b0] offset:36864\n"
      "ds_read_b64_tr_b16 v[50:51], %[b0] offset:37632\n"
      "ds_read_b64_tr_b16 v[52:53], %[b0] offset:36928\n"
      "ds_read_b64_tr_b16 v[54:55], %[b0] offset:37696\n"
      "ds_read_b64_tr_b16 v[56:57], %[b0] offset:36992\n"
      "ds_read_b64_tr_b16 v[58:59], %[b0] offset:37760\n"
      "ds_read_b64_tr_b16 v[60:61], %[b1] offset:36864\n"
      "ds_read_b64_tr_b16 v[62:63], %[b1] offset:37632\n"
      "ds_read_b64_tr_b16 v[64:65], %[b1] offset:36928\n"
      "ds_read_b64_tr_b16 v[66:67], %[b1] offset:37696\n"
      "ds_read_b64_tr_b16 v[68:69], %[b1] offset:36992\n"
      "ds_read_b64_tr_b16 v[70:71], %[b1] offset:37760\n"
      "ds_read_b64_tr_b16 v[72:73], %[b2] offset:36864\n"
      "ds_read_b64_tr_b16 v[74:75], %[b2] offset:37632\n"
      "s_waitcnt lgkmcnt(10)\n"
      "v_mfma_f32_32x32x16_bf16 v[80:95], v[176:179], v[48:51], v[80:95]\n"
      "ds_read_b64_tr_b16 v[76:77], %[b2] offset:36928\n"
      "ds_read_b64_tr_b16 v[78:79], %[b2] offset:37696\n"
      "v_lshlrev_b32 %[t0], 16, v48\n"
      "v_and_b32 %[t1], %[msk], v48\n"
      "v_add_f32 %[c0], %[c0], %[t0]\n"
      "ds_read_b128 v[192:195], %[a0] offset:0\n"
      "ds_read_b128 v[196:199], %[a1] offset:0\n"
      "v_add_f32 %[c0], %[c0], %[t1]\n"
      "v_mfma_f32_32x32x16_bf16 v[96:111], v[176:179], v[52:55], v[96:111]\n"
      "ds_read_b64_tr_b16 v[48:49], %[b2] offset:36992\n"
      "ds_read_b64_tr_b16 v[50:51], %[b2] offset:37760\n"
      "v_lshlrev_b32 %[t0], 16, v52\n"
      "v_and_b32 %[t1], %[msk], v52\n"
      "v_add_f32 %[c1], %[c1], %[t0]\n"
      "ds_read_b128 v[200:203], %[a2] offset:0\n"
      "ds_read_b128 v[204:207], %[a3] offset:0\n"
      "v_add_f32 %[c1], %[c1], %[t1]\n"
      "s_waitcnt lgkmcnt(14)\n"
      "v_mfma_f32_32x32x16_bf16 v[112:127], v[176:179], v[56:59], v[112:127]\n"
      "ds_read_b64_tr_b16 v[52:53], %[b3] offset:36864\n"
      "ds_read_b64_tr_b16 v[54:55], %[b3] offset:37632\n"
      "v_lshlrev_b32 %[t0], 16, v56\n"
      "v_and_b32 %[t1], %[msk], v56\n"
      "v_add_f32 %[c2], %[c2], %[t0]\n"
      "ds_read_b128 v[208:211], %[a4] offset:0\n"
      "v_add_f32 %[c2], %[c2], %[t1]\n"
      "v_mfma_f32_32x32x16_bf16 v[80:95], v[180:183], v[60:63], v[80:95]\n"
      "ds_read_b64_tr_b16 v[56:57], %[b3] offset:36928\n"
      "ds_read_b64_tr_b16 v[58:59], %[b3] offset:37696\n"
      "v_lshlrev_b32 %[t0], 16, v61\n"
      "v_and_b32 %[t1], %[msk], v61\n"
      "v_add_f32 %[c0], %[c0], %[t0]\n"
      "ds_read_b128 v[212:215], %[a5] offset:0\n"
      "ds_read_b128 v[216:219], %[a6] offset:0\n"
      "v_add_f32 %[c0], %[c0], %[t1]\n"
      "s_waitcnt lgkmcnt(15)\n"
      "v_mfma_f32_32x32x16_bf16 v[96:111], v[180:183], v[64:67], v[96:111]\n"
      "ds_read_b64_tr_b16 v[60:61], %[b3] offset:36992\n"
      "ds_read_b64_tr_b16 v[62:63], %[b3] offset:37760\n"
      "v_lshlrev_b32 %[t0], 16, v65\n"
      "v_and_b32 %[t1], %[msk], v65\n"
      "v_add_f32 %[c1], %[c1], %[t0]\n"
      "ds_read_b128 v[220:223], %[a7] offset:0\n"
      "v_add_f32 %[c1], %[c1], %[t1]\n"
      "v_mfma_f32_32x32x16_bf16 v[112:127], v[180:183], v[68:71], v[112:127]\n"
      "v_lshlrev_b32 %[t0], 16, v69\n"
      "v_and_b32 %[t1], %[msk], v69\n"
      "v_add_f32 %[c2], %[c2], %[t0]\n"
      "ds_read_b128 v[224:227], %[a0] offset:256\n"
      "ds_read_b128 v[228:231], %[a1] offset:256\n"
      "v_add_f32 %[c2], %[c2], %[t1]\n"
      "s_waitcnt lgkmcnt(15)\n"
      "v_mfma_f32_32x32x16_bf16 v[80:95], v[184:187], v[72:75], v[80:95]\n"
      "v_lshlrev_b32 %[t0], 16, v74\n"
      "v_and_b32 %[t1], %[msk], v74\n"
      "v_add_f32 %[c0], %[c0], %[t0]\n"
      "ds_read_b128 v[232:235], %[a2] offset:256\n"
      "ds_read_b128 v[236:239], %[a3] offset:256\n"
      "v_add_f32 %[c0], %[c0], %[t1]\n"
      "v_mfma_f32_32x32x16_bf16 v[96:111], v[184:187], v[76:79], v[96:111]\n"
      "v_lshlrev_b32 %[t0], 16, v78\n"
      "v_and_b32 %[t1], %[msk], v78\n"
      "v_add_f32 %[c1], %[c1], %[t0]\n"
      "ds_read_b128 v[240:243], %[a4] offset:256\n"
      "v_add_f32 %[c1], %[c1], %[t1]\n"
      "s_waitcnt lgkmcnt(13)\n"
      "v_mfma_f32_32x32x16_bf16 v[112:127], v[184:187], v[48:51], v[112:127]\n"
      "v_lshlrev_b32 %[t0], 16, v50\n"
      "v_and_b32 %[t1], %[msk], v50\n"
      "v_add_f32 %[c2], %[c2], %[t0]\n"
      "ds_read_b128 v[244:247], %[a5] offset:256\n"
      "ds_read_b128 v[248:251], %[a6] offset:256\n"
      "v_add_f32 %[c2], %[c2], %[t1]\n"
      "v_mfma_f32_32x32x16_bf16 v[80:95], v[188:191], v[52:55], v[80:95]\n"
      "v_lshlrev_b32 %[t0], 16, v55\n"
      "v_and_b32 %[t1], %[msk], v55\n"
      "v_add_f32 %[c0], %[c0], %[t0]\n"
      "ds_read_b128 v[252:255], %[a7] offset:256\n"
      "v_add_f32 %[c0], %[c0], %[t1]\n"
      "s_waitcnt lgkmcnt(9)\n"
      "v_mfma_f32_32x32x16_bf16 v[96:111], v[188:191], v[56:59], v[96:111]\n"
      "v_lshlrev_b32 %[t0], 16, v59\n"
      "v_and_b32 %[t1], %[msk], v59\n"
      "v_add_f32 %[c1], %[c1], %[t0]\n"
      "v_add_f32 %[c1], %[c1], %[t1]\n"
      "v_mfma_f32_32x32x16_bf16 v[112:127], v[188:191], v[60:63], v[112:127]\n"
      "v_lshlrev_b32 %[t0], 16, v63\n"
      "v_and_b32 %[t1], %[msk], v63\n"
      "v_add_f32 %[c2], %[c2], %[t0]\n"
      "v_add_f32 %[c2], %[c2], %[t1]\n"
      "s_waitcnt lgkmcnt(0)\n"
      "s_nop 15\n"
      "3:\n"
        : "+{v[80:95]}"(a0), "+{v[96:111]}"(a1), "+{v[112:127]}"(a2), CSUM, "=&{v[192:207]}"(X[4]), "=&{v[208:223]}"(X[5]), "=&{v[224:239]}"(X[6]), "=&{v[240:255]}"(X[7])
        : "{v[176:191]}"(X[3]), B4, XADR, [msk] "s"(msk), [cg] "s"(cg) : QCLOB, "scc", "memory");
#pragma unroll
    for (int i = 0; i < 4; i++) wb[i] += 49152;
    BAR();
    asm volatile("global_load_dwordx2 %0, %1, %2 sc1" : "=v"(rsw) : "v"(rso), "s"(ws) : "memory");
    asm volatile(
      "s_cmp_gt_u32 %[cg], 10\n"
      "s_cbranch_scc1 2f\n"
      "s_cmp_eq_u32 %[cg], 10\n"
      "s_cbranch_scc1 1f\n"
      "ds_read_b64_tr_b16 v[48:49], %[b0] offset:0\n"
      "ds_read_b64_tr_b16 v[50:51], %[b0] offset:768\n"
      "ds_read_b64_tr_b16 v[52:53], %[b0] offset:64\n"
      "ds_read_b64_tr_b16 v[54:55], %[b0] offset:832\n"
      "ds_read_b64_tr_b16 v[56:57], %[b0] offset:128\n"
      "ds_read_b64_tr_b16 v[58:59], %[b0] offset:896\n"
      "ds_read_b64_tr_b16 v[60:61], %[b1] offset:0\n"
      "ds_read_b64_tr_b16 v[62:63], %[b1] offset:768\n"
      "ds_read_b64_tr_b16 v[64:65], %[b1] offset:64\n"
      "ds_read_b64_tr_b16 v[66:67], %[b1] offset:832\n"
      "ds_read_b64_tr_b16 v[68:69], %[b1] offset:128\n"
      "ds_read_b64_tr_b16 v[70:71], %[b1] offset:896\n"
      "ds_read_b64_tr_b16 v[72:73], %[b2] offset:0\n"
      "ds_read_b64_tr_b16 v[74:75], %[b2] offset:768\n"
      "s_waitcnt lgkmcnt(10)\n"
      "v_mfma_f32_32x32x16_bf16 v[80:95], v[48:51], v[192:195], v[80:95]\n"
      "ds_read_b64_tr_b16 v[76:77], %[b2] offset:64\n"
      "ds_read_b64_tr_b16 v[78:79], %[b2] offset:832\n"
      "v_lshlrev_b32 %[t0], 16, v48\n"
      "v_and_b32 %[t1], %[msk], v48\n"
      "v_add_f32 %[c0], %[c0], %[t0]\n"
      "v_add_f32 %[c0], %[c0], %[t1]\n"
      "v_mfma_f32_32x32x16_bf16 v[96:111], v[52:55], v[192:195], v[96:111]\n"
      "ds_read_b64_tr_b16 v[48:49], %[b2] offset:128\n"
      "ds_read_b64_tr_b16 v[50:51], %[b2] offset:896\n"
      "v_lshlrev_b32 %[t0], 16, v52\n"
      "v_and_b32 %[t1], %[msk], v52\n"
      "v_add_f32 %[c1], %[c1], %[t0]\n"
      "v_add_f32 %[c1], %[c1], %[t1]\n"
      "s_waitcnt lgkmcnt(10)\n"
      "v_mfma_f32_32x32x16_bf16 v[112:127], v[56:59], v[192:195], v[112:127]\n"
      "ds_read_b64_tr_b16 v[52:53], %[b3] offset:0\n"
      "ds_read_b64_tr_b16 v[54:55], %[b3] offset:768\n"
      "v_lshlrev_b32 %[t0], 16, v56\n"
      "v_and_b32 %[t1], %[msk], v56\n"
      "v_add_f32 %[c2], %[c2], %[t0]\n"
      "v_add_f32 %[c2], %[c2], %[t1]\n"
      "v_mfma_f32_32x32x16_bf16 v[80:95], v[60:63], v[196:199], v[80:95]\n"
      "ds_read_b64_tr_b16 v[56:57], %[b3] offset:64\n"
      "ds_read_b64_tr_b16 v[58:59], %[b3] offset:832\n"
      "v_lshlrev_b32 %[t0], 16, v61\n"
      "v_and_b32 %[t1], %[msk], v61\n"
      "v_add_f32 %[c0], %[c0], %[t0]\n"
      "v_add_f32 %[c0], %[c0], %[t1]\n"
      "s_waitcnt lgkmcnt(10)\n"
      "v_mfma_f32_32x32x16_bf16 v[96:111], v[64:67], v[196:199], v[96:111]\n"
      "ds_read_b64_tr_b16 v[60:61], %[b3] offset:128\n"
      "ds_read_b64_tr_b16 v[62:63], %[b3] offset:896\n"
      "v_lshlrev_b32 %[t0], 16, v65\n"
      "v_and_b32 %[t1], %[msk], v65\n"
      "v_add_f32 %[c1], %[c1], %[t0]\n"
      "v_add_f32 %[c1], %[c1], %[t1]\n"
      "v_mfma_f32_32x32x16_bf16 v[112:127], v[68:71], v[196:199], v[112:127]\n"
      "ds_read_b64_tr_b16 v[64:65], %[b0] offset:12288\n"
      "ds_read_b64_tr_b16 v[66:67], %[b0] offset:13056\n"
      "v_lshlrev_b32 %[t0], 16, v69\n"
      "v_and_b32 %[t1], %[msk], v69\n"
      "v_add_f32 %[c2], %[c2], %[t0]\n"
      "v_add_f32 %[c2], %[c2], %[t1]\n"
      "s_waitcnt lgkmcnt(10)\n"
      "v_mfma_f32_32x32x16_bf16 v[80:95], v[72:75], v[200:203], v[80:95]\n"
      "ds_read_b64_tr_b16 v[68:69], %[b0] offset:12352\n"
      "ds_read_b64_tr_b16 v[70:71], %[b0] offset:13120\n"
      "v_lshlrev_b32 %[t0], 16, v74\n"
      "v_and_b32 %[t1], %[msk], v74\n"
      "v_add_f32 %[c0], %[c0], %[t0]\n"
      "v_add_f32 %[c0], %[c0], %[t1]\n"
      "v_mfma_f32_32x32x16_bf16 v[96:111], v[76:79], v[200:203], v[96:111]\n"
      "ds_read_b64_tr_b16 v[72:73], %[b0] offset:12416\n"
      "ds_read_b64_tr_b16 v[74:75], %[b0] offset:13184\n"
      "v_lshlrev_b32 %[t0], 16, v78\n"
      "v_and_b32 %[t1], %[msk], v78\n"
      "v_add_f32 %[c1], %[c1], %[t0]\n"
      "v_add_f32 %[c1], %[c1], %[t1]\n"
      "s_waitcnt lgkmcnt(10)\n"
      "v_mfma_f32_32x32x16_bf16 v[112:127], v[48:51], v[200:203], v[112:127]\n"
      "ds_read_b64_tr_b16 v[76:77], %[b1] offset:12288\n"
      "ds_read_b64_tr_b16 v[78:79], %[b1] offset:13056\n"
      "v_lshlrev_b32 %[t0], 16, v50\n"
      "v_and_b32 %[t1], %[msk], v50\n"
      "v_add_f32 %[c2], %[c2], %[t0]\n"
      "v_add_f32 %[c2], %[c2], %[t1]\n"
      "v_mfma_f32_32x32x16_bf16 v[80:95], v[52:55], v[204:207], v[80:95]\n"
      "ds_read_b64_tr_b16 v[48:49], %[b1] offset:12352\n"
      "ds_read_b64_tr_b16 v[50:51], %[b1] offset:13120\n"
      "v_lshlrev_b32 %[t0], 16, v55\n"
      "v_and_b32 %[t1], %[msk], v55\n"
      "v_add_f32 %[c0], %[c0], %[t0]\n"
      "v_add_f32 %[c0], %[c0], %[t1]\n"
      "s_waitcnt lgkmcnt(10)\n"
      "v_mfma_f32_32x32x16_bf16 v[96:111], v[56:59], v[204:207], v[96:111]\n"
      "ds_read_b64_tr_b16 v[52:53], %[b1] offset:12416\n"
      "ds_read_b64_tr_b16 v[54:55], %[b1] offset:13184\n"
      "v_lshlrev_b32 %[t0], 16, v59\n"
      "v_and_b32 %[t1], %[msk], v59\n"
      "v_add_f32 %[c1], %[c1], %[t0]\n"
      "v_add_f32 %[c1], %[c1], %[t1]\n"
      "v_mfma_f32_32x32x16_bf16 v[112:127], v[60:63], v[204:207], v[112:127]\n"
      "ds_read_b64_tr_b16 v[56:57], %[b2] offset:12288\n"
      "ds_read_b64_tr_b16 v[58:59], %[b2] offset:13056\n"
      "v_lshlrev_b32 %[t0], 16, v63\n"
      "v_and_b32 %[t1], %[msk], v63\n"
      "v_add_f32 %[c2], %[c2], %[t0]\n"
      "v_add_f32 %[c2], %[c2], %[t1]\n"
      "s_waitcnt lgkmcnt(10)\n"
      "v_mfma_f32_32x32x16_bf16 v[80:95], v[64:67], v[208:211], v[80:95]\n"
      "ds_read_b64_tr_b16 v[60:61], %[b2] offset:12352\n"
      "ds_read_b64_tr_b16 v[62:63], %[b2] offset:13120\n"
      "v_lshlrev_b32 %[t0], 16, v64\n"
      "v_and_b32 %[t1], %[msk], v64\n"
      "v_add_f32 %[c0], %[c0], %[t0]\n"
      "v_add_f32 %[c0], %[c0], %[t1]\n"
      "v_mfma_f32_32x32x16_bf16 v[96:111], v[68:71], v[208:211], v[96:111]\n"
      "ds_read_b64_tr_b16 v[64:65], %[b2] offset:12416\n"
      "ds_read_b64_tr_b16 v[66:67], %[b2] offset:13184\n"
      "v_lshlrev_b32 %[t0], 16, v68\n"
      "v_and_b32 %[t1], %[msk], v68\n"
      "v_add_f32 %[c1], %[c1], %[t0]\n"
      "v_add_f32 %[c1], %[c1], %[t1]\n"
      "s_waitcnt lgkmcnt(10)\n"
      "v_mfma_f32_32x32x16_bf16 v[112:127], v[72:75], v[208:211], v[112:127]\n"
      "ds_read_b64_tr_b16 v[68:69], %[b3] offset:12288\n"
      "ds_read_b64_tr_b16 v[70:71], %[b3] offset:13056\n"
      "v_lshlrev_b32 %[t0], 16, v72\n"
      "v_and_b32 %[t1], %[msk], v72\n"
      "v_add_f32 %[c2], %[c2], %[t0]\n"
      "v_add_f32 %[c2], %[c2], %[t1]\n"
      "v_mfma_f32_32x32x16_bf16 v[80:95], v[76:79], v[212:215], v[80:95]\n"
      "ds_read_b64_tr_b16 v[72:73], %[b3] offset:12352\n"
      "ds_read_b64_tr_b16 v[74:75], %[b3] offset:13120\n"
      "v_lshlrev_b32 %[t0], 16, v77\n"
      "v_and_b32 %[t1], %[msk], v77\n"
      "v_add_f32 %[c0], %[c0], %[t0]\n"
      "v_add_f32 %[c0], %[c0], %[t1]\n"
      "s_waitcnt lgkmcnt(10)\n"
      "v_mfma_f32_32x32x16_bf16 v[96:111], v[48:51], v[212:215], v[96:111]\n"
      "ds_read_b64_tr_b16 v[76:77], %[b3] offset:12416\n"
      "ds_read_b64_tr_b16 v[78:79], %[b3] offset:13184\n"
      "v_lshlrev_b32 %[t0], 16, v49\n"
      "v_and_b32 %[t1], %[msk], v49\n"
      "v_add_f32 %[c1], %[c1], %[t0]\n"
      "v_add_f32 %[c1], %[c1], %[t1]\n"
      "v_mfma_f32_32x32x16_bf16 v[112:127], v[52:55], v[212:215], v[112:127]\n"
      "ds_read_b64_tr_b16 v[48:49], %[b0] offset:24576\n"
      "ds_read_b64_tr_b16 v[50:51], %[b0] offset:25344\n"
      "v_lshlrev_b32 %[t0], 16, v53\n"
      "v_and_b32 %[t1], %[msk], v53\n"
      "v_add_f32 %[c2], %[c2], %[t0]\n"
      "v_add_f32 %[c2], %[c2], %[t1]\n"
      "s_waitcnt lgkmcnt(10)\n"
      "v_mfma_f32_32x32x16_bf16 v[80:95], v[56:59], v[216:219], v[80:95]\n"
      "ds_read_b64_tr_b16 v[52:53], %[b0] offset:24640\n"
      "ds_read_b64_tr_b16 v[54:55], %[b0] offset:25408\n"
      "v_lshlrev_b32 %[t0], 16, v58\n"
      "v_and_b32 %[t1], %[msk], v58\n"
      "v_add_f32 %[c0], %[c0], %[t0]\n"
      "v_add_f32 %[c0], %[c0], %[t1]\n"
      "v_mfma_f32_32x32x16_bf16 v[96:111], v[60:63], v[216:219], v[96:111]\n"
      "ds_read_b64_tr_b16 v[56:57], %[b0] offset:24704\n"
      "ds_read_b64_tr_b16 v[58:59], %[b0] offset:25472\n"
      "v_lshlrev_b32 %[t0], 16, v62\n"
      "v_and_b32 %[t1], %[msk], v62\n"
      "v_add_f32 %[c1], %[c1], %[t0]\n"
      "v_add_f32 %[c1], %[c1], %[t1]\n"
      "s_waitcnt lgkmcnt(10)\n"
      "v_mfma_f32_32x32x16_bf16 v[112:127], v[64:67], v[216:219], v[112:127]\n"
      "ds_read_b64_tr_b16 v[60:61], %[b1] offset:24576\n"
      "ds_read_b64_tr_b16 v[62:63], %[b1] offset:25344\n"
      "v_lshlrev_b32 %[t0], 16, v66\n"
      "v_and_b32 %[t1], %[msk], v66\n"
      "v_add_f32 %[c2], %[c2], %[t0]\n"
      "v_add_f32 %[c2], %[c2], %[t1]\n"
      "v_mfma_f32_32x32x16_bf16 v[80:95], v[68:71], v[220:223], v[80:95]\n"
      "ds_read_b64_tr_b16 v[64:65], %[b1] offset:24640\n"
      "ds_read_b64_tr_b16 v[66:67], %[b1] offset:25408\n"
      "v_lshlrev_b32 %[t0], 16, v71\n"
      "v_and_b32 %[t1], %[msk], v71\n"
      "v_add_f32 %[c0], %[c0], %[t0]\n"
      "v_add_f32 %[c0], %[c0], %[t1]\n"
      "s_waitcnt lgkmcnt(10)\n"
      "v_mfma_f32_32x32x16_bf16 v[96:111], v[72:75], v[220:223], v[96:111]\n"
      "ds_read_b64_tr_b16 v[68:69], %[b1] offset:24704\n"
      "ds_read_b64_tr_b16 v[70:71], %[b1] offset:25472\n"
      "v_lshlrev_b32 %[t0], 16, v75\n"
      "v_and_b32 %[t1], %[msk], v75\n"
      "v_add_f32 %[c1], %[c1], %[t0]\n"
      "v_add_f32 %[c1], %[c1], %[t1]\n"
      "v_mfma_f32_32x32x16_bf16 v[112:127], v[76:79], v[220:223], v[112:127]\n"
      "ds_read_b64_tr_b16 v[72:73], %[b2] offset:24576\n"
      "ds_read_b64_tr_b16 v[74:75], %[b2] offset:25344\n"
      "v_lshlrev_b32 %[t0], 16, v79\n"
      "v_and_b32 %[t1], %[msk], v79\n"
      "v_add_f32 %[c2], %[c2], %[t0]\n"
      "v_add_f32 %[c2], %[c2], %[t1]\n"
      "s_waitcnt lgkmcnt(10)\n"
      "v_mfma_f32_32x32x16_bf16 v[80:95], v[48:51], v[224:227], v[80:95]\n"
      "ds_read_b64_tr_b16 v[76:77], %[b2] offset:24640\n"
      "ds_read_b64_tr_b16 v[78:79], %[b2] offset:25408\n"
      "v_lshlrev_b32 %[t0], 16, v48\n"
      "v_and_b32 %[t1], %[msk], v48\n"
      "v_add_f32 %[c0], %[c0], %[t0]\n"
      "v_add_f32 %[c0], %[c0], %[t1]\n"
      "v_mfma_f32_32x32x16_bf16 v[96:111], v[52:55], v[224:227], v[96:111]\n"
      "ds_read_b64_tr_b16 v[48:49], %[b2] offset:24704\n"
      "ds_read_b64_tr_b16 v[50:51], %[b2] offset:25472\n"
      "v_lshlrev_b32 %[t0], 16, v52\n"
      "v_and_b32 %[t1], %[msk], v52\n"
      "v_add_f32 %[c1], %[c1], %[t0]\n"
      "v_add_f32 %[c1], %[c1], %[t1]\n"
      "s_waitcnt lgkmcnt(10)\n"
      "v_mfma_f32_32x32x16_bf16 v[112:127], v[56:59], v[224:227], v[112:127]\n"
      "ds_read_b64_tr_b16 v[52:53], %[b3] offset:24576\n"
      "ds_read_b64_tr_b16 v[54:55], %[b3] offset:25344\n"
      "v_lshlrev_b32 %[t0], 16, v56\n"
      "v_and_b32 %[t1], %[msk], v56\n"
      "v_add_f32 %[c2], %[c2], %[t0]\n"
      "v_add_f32 %[c2], %[c2], %[t1]\n"
      "v_mfma_f32_32x32x16_bf16 v[80:95], v[60:63], v[228:231], v[80:95]\n"
      "ds_read_b64_tr_b16 v[56:57], %[b3] offset:24640\n"
      "ds_read_b64_tr_b16 v[58:59], %[b3] offset:25408\n"
      "v_lshlrev_b32 %[t0], 16, v61\n"
      "v_and_b32 %[t1], %[msk], v61\n"
      "v_add_f32 %[c0], %[c0], %[t0]\n"
      "v_add_f32 %[c0], %[c0], %[t1]\n"
      "s_waitcnt lgkmcnt(10)\n"
      "v_mfma_f32_32x32x16_bf16 v[96:111], v[64:67], v[228:231], v[96:111]\n"
      "ds_read_b64_tr_b16 v[60:61], %[b3] offset:24704\n"
      "ds_read_b64_tr_b16 v[62:63], %[b3] offset:25472\n"
      "v_lshlrev_b32 %[t0], 16, v65\n"
      "v_and_b32 %[t1], %[msk], v65\n"
      "v_add_f32 %[c1], %[c1], %[t0]\n"
      "v_add_f32 %[c1], %[c1], %[t1]\n"
      "v_mfma_f32_32x32x16_bf16 v[112:127], v[68:71], v[228:231], v[112:127]\n"
      "ds_read_b64_tr_b16 v[64:65], %[b0] offset:36864\n"
      "ds_read_b64_tr_b16 v[66:67], %[b0] offset:37632\n"
      "v_lshlrev_b32 %[t0], 16, v69\n"
      "v_and_b32 %[t1], %[msk], v69\n"
      "v_add_f32 %[c2], %[c2], %[t0]\n"
      "v_add_f32 %[c2], %[c2], %[t1]\n"
      "s_waitcnt lgkmcnt(10)\n"
      "v_mfma_f32_32x32x16_bf16 v[80:95], v[72:75], v[232:235], v[80:95]\n"
      "ds_read_b64_tr_b16 v[68:69], %[b0] offset:36928\n"
      "ds_read_b64_tr_b16 v[70:71], %[b0] offset:37696\n"
      "v_lshlrev_b32 %[t0], 16, v74\n"
      "v_and_b32 %[t1], %[msk], v74\n"
      "v_add_f32 %[c0], %[c0], %[t0]\n"
      "v_add_f32 %[c0], %[c0], %[t1]\n"
      "v_mfma_f32_32x32x16_bf16 v[96:111], v[76:79], v[232:235], v[96:111]\n"
      "ds_read_b64_tr_b16 v[72:73], %[b0] offset:36992\n"
      "ds_read_b64_tr_b16 v[74:75], %[b0] offset:37760\n"
      "v_lshlrev_b32 %[t0], 16, v78\n"
      "v_and_b32 %[t1], %[msk], v78\n"
      "v_add_f32 %[c1], %[c1], %[t0]\n"
      "v_add_f32 %[c1], %[c1], %[t1]\n"
      "s_waitcnt lgkmcnt(10)\n"
      "v_mfma_f32_32x32x16_bf16 v[112:127], v[48:51], v[232:235], v[112:127]\n"
      "ds_read_b64_tr_b16 v[76:77], %[b1] offset:36864\n"
      "ds_read_b64_tr_b16 v[78:79], %[b1] offset:37632\n"
      "v_lshlrev_b32 %[t0], 16, v50\n"
      "v_and_b32 %[t1], %[msk], v50\n"
      "v_add_f32 %[c2], %[c2], %[t0]\n"
      "v_add_f32 %[c2], %[c2], %[t1]\n"
      "v_mfma_f32_32x32x16_bf16 v[80:95], v[52:55], v[236:239], v[80:95]\n"
      "ds_read_b64_tr_b16 v[48:49], %[b1] offset:36928\n"
      "ds_read_b64_tr_b16 v[50:51], %[b1] offset:37696\n"
      "v_lshlrev_b32 %[t0], 16, v55\n"
      "v_and_b32 %[t1], %[msk], v55\n"
      "v_add_f32 %[c0], %[c0], %[t0]\n"
      "v_add_f32 %[c0], %[c0], %[t1]\n"
      "s_waitcnt lgkmcnt(10)\n"
      "v_mfma_f32_32x32x16_bf16 v[96:111], v[56:59], v[236:239], v[96:111]\n"
      "ds_read_b64_tr_b16 v[52:53], %[b1] offset:36992\n"
      "ds_read_b64_tr_b16 v[54:55], %[b1] offset:37760\n"
      "v_lshlrev_b32 %[t0], 16, v59\n"
      "v_and_b32 %[t1], %[msk], v59\n"
      "v_add_f32 %[c1], %[c1], %[t0]\n"
      "v_add_f32 %[c1], %[c1], %[t1]\n"
      "v_mfma_f32_32x32x16_bf16 v[112:127], v[60:63], v[236:239], v[112:127]\n"
      "ds_read_b64_tr_b16 v[56:57], %[b2] offset:36864\n"
      "ds_read_b64_tr_b16 v[58:59], %[b2] offset:37632\n"
      "v_lshlrev_b32 %[t0], 16, v63\n"
      "v_and_b32 %[t1], %[msk], v63\n"
      "v_add_f32 %[c2], %[c2], %[t0]\n"
      "v_add_f32 %[c2], %[c2], %[t1]\n"
      "s_waitcnt lgkmcnt(10)\n"
      "v_mfma_f32_32x32x16_bf16 v[80:95], v[64:67], v[240:243], v[80:95]\n"
      "ds_read_b64_tr_b16 v[60:61], %[b2] offset:36928\n"
      "ds_read_b64_tr_b16 v[62:63], %[b2] offset:37696\n"
      "v_lshlrev_b32 %[t0], 16, v64\n"
      "v_and_b32 %[t1], %[msk], v64\n"
      "v_add_f32 %[c0], %[c0], %[t0]\n"
      "v_add_f32 %[c0], %[c0], %[t1]\n"
      "v_mfma_f32_32x32x16_bf16 v[96:111], v[68:71], v[240:243], v[96:111]\n"
      "ds_read_b64_tr_b16 v[64:65], %[b2] offset:36992\n"
      "ds_read_b64_tr_b16 v[66:67], %[b2] offset:37760\n"
      "v_lshlrev_b32 %[t0], 16, v68\n"
      "v_and_b32 %[t1], %[msk], v68\n"
      "v_add_f32 %[c1], %[c1], %[t0]\n"
      "v_add_f32 %[c1], %[c1], %[t1]\n"
      "s_waitcnt lgkmcnt(10)\n"
      "v_mfma_f32_32x32x16_bf16 v[112:127], v[72:75], v[240:243], v[112:127]\n"
      "ds_read_b64_tr_b16 v[68:69], %[b3] offset:36864\n"
      "ds_read_b64_tr_b16 v[70:71], %[b3] offset:37632\n"
      "v_lshlrev_b32 %[t0], 16, v72\n"
      "v_and_b32 %[t1], %[msk], v72\n"
      "v_add_f32 %[c2], %[c2], %[t0]\n"
      "v_add_f32 %[c2], %[c2], %[t1]\n"
      "v_mfma_f32_32x32x16_bf16 v[80:95], v[76:79], v[244:247], v[80:95]\n"
      "ds_read_b64_tr_b16 v[72:73], %[b3] offset:36928\n"
      "ds_read_b64_tr_b16 v[74:75], %[b3] offset:37696\n"
      "v_lshlrev_b32 %[t0], 16, v77\n"
      "v_and_b32 %[t1], %[msk], v77\n"
      "v_add_f32 %[c0], %[c0], %[t0]\n"
      "v_add_f32 %[c0], %[c0], %[t1]\n"
      "s_waitcnt lgkmcnt(10)\n"
      "v_mfma_f32_32x32x16_bf16 v[96:111], v[48:51], v[244:247], v[96:111]\n"
      "ds_read_b64_tr_b16 v[76:77], %[b3] offset:36992\n"
      "ds_read_b64_tr_b16 v[78:79], %[b3] offset:37760\n"
      "v_lshlrev_b32 %[t0], 16, v49\n"
      "v_and_b32 %[t1], %[msk], v49\n"
      "v_add_f32 %[c1], %[c1], %[t0]\n"
      "v_add_f32 %[c1], %[c1], %[t1]\n"
      "v_mfma_f32_32x32x16_bf16 v[112:127], v[52:55], v[244:247], v[112:127]\n"
      "v_lshlrev_b32 %[t0], 16, v53\n"
      "v_and_b32 %[t1], %[msk], v53\n"
      "v_add_f32 %[c2], %[c2], %[t0]\n"
      "v_add_f32 %[c2], %[c2], %[t1]\n"
      "s_waitcnt lgkmcnt(8)\n"
      "v_mfma_f32_32x32x16_bf16 v[80:95], v[56:59], v[248:251], v[80:95]\n"
      "v_lshlrev_b32 %[t0], 16, v58\n"
      "v_and_b32 %[t1], %[msk], v58\n"
      "v_add_f32 %[c0], %[c0], %[t0]\n"
      "v_add_f32 %[c0], %[c0], %[t1]\n"
      "v_mfma_f32_32x32x16_bf16 v[96:111], v[60:63], v[248:251], v[96:111]\n"
      "v_lshlrev_b32 %[t0], 16, v62\n"
      "v_and_b32 %[t1], %[msk], v62\n"
      "v_add_f32 %[c1], %[c1], %[t0]\n"
      "v_add_f32 %[c1], %[c1], %[t1]\n"
      "s_waitcnt lgkmcnt(4)\n"
      "v_mfma_f32_32x32x16_bf16 v[112:127], v[64:67], v[248:251], v[112:127]\n"
      "v_lshlrev_b32 %[t0], 16, v66\n"
      "v_and_b32 %[t1], %[msk], v66\n"
      "v_add_f32 %[c2], %[c2], %[t0]\n"
      "v_add_f32 %[c2], %[c2], %[t1]\n"
      "v_mfma_f32_32x32x16_bf16 v[80:95], v[68:71], v[252:255], v[80:95]\n"
      "v_lshlrev_b32 %[t0], 16, v71\n"
      "v_and_b32 %[t1], %[msk], v71\n"
      "v_add_f32 %[c0], %[c0], %[t0]\n"
      "v_add_f32 %[c0], %[c0], %[t1]\n"
      "s_waitcnt lgkmcnt(0)\n"
      "v_mfma_f32_32x32x16_bf16 v[96:111], v[72:75], v[252:255], v[96:111]\n"
      "v_lshlrev_b32 %[t0], 16, v75\n"
      "v_and_b32 %[t1], %[msk], v75\n"
      "v_add_f32 %[c1], %[c1], %[t0]\n"
      "v_add_f32 %[c1], %[c1], %[t1]\n"
      "v_mfma_f32_32x32x16_bf16 v[112:127], v[76:79], v[252:255], v[112:127]\n"
      "v_lshlrev_b32 %[t0], 16, v79\n"
      "v_and_b32 %[t1], %[msk], v79\n"
      "v_add_f32 %[c2], %[c2], %[t0]\n"
      "v_add_f32 %[c2], %[c2], %[t1]\n"
      "s_nop 15\n"
      "s_branch 3f\n"
      "1:\n"
      "ds_read_b64_tr_b16 v[48:49], %[b0] offset:0\n"
      "ds_read_b64_tr_b16 v[50:51], %[b0] offset:768\n"
      "ds_read_b64_tr_b16 v[52:53], %[b0] offset:64\n"
      "ds_read_b64_tr_b16 v[54:55], %[b0] offset:832\n"
      "ds_read_b64_tr_b16 v[56:57], %[b0] offset:128\n"
      "ds_read_b64_tr_b16 v[58:59], %[b0] offset:896\n"
      "ds_read_b64_tr_b16 v[60:61], %[b1] offset:0\n"
      "ds_read_b64_tr_b16 v[62:63], %[b1] offset:768\n"
      "ds_read_b64_tr_b16 v[64:65], %[b1] offset:64\n"
      "ds_read_b64_tr_b16 v[66:67], %[b1] offset:832\n"
      "ds_read_b64_tr_b16 v[68:69], %[b1] offset:128\n"
      "ds_read_b64_tr_b16 v[70:71], %[b1] offset:896\n"
      "ds_read_b64_tr_b16 v[72:73], %[b2] offset:0\n"
      "ds_read_b64_tr_b16 v[74:75], %[b2] offset:768\n"
      "s_waitcnt lgkmcnt(10)\n"
      "v_mfma_f32_32x32x16_bf16 v[80:95], v[48:51], v[192:195], v[80:95]\n"
      "ds_read_b64_tr_b16 v[76:77], %[b2] offset:64\n"
      "ds_read_b64_tr_b16 v[78:79], %[b2] offset:832\n"
      "v_lshlrev_b32 %[t0], 16, v48\n"
      "v_and_b32 %[t1], %[msk], v48\n"
      "v_add_f32 %[c0], %[c0], %[t0]\n"
      "v_add_f32 %[c0], %[c0], %[t1]\n"
      "v_mfma_f32_32x32x16_bf16 v[96:111], v[52:55], v[192:195], v[96:111]\n"
      "ds_read_b64_tr_b16 v[48:49], %[b2] offset:128\n"
      "ds_read_b64_tr_b16 v[50:51], %[b2] offset:896\n"
      "v_lshlrev_b32 %[t0], 16, v52\n"
      "v_and_b32 %[t1], %[msk], v52\n"
      "v_add_f32 %[c1], %[c1], %[t0]\n"
      "v_add_f32 %[c1], %[c1], %[t1]\n"
      "s_waitcnt lgkmcnt(10)\n"
      "v_mfma_f32_32x32x16_bf16 v[112:127], v[192:195], v[56:59], v[112:127]\n"
      "ds_read_b64_tr_b16 v[52:53], %[b3] offset:0\n"
      "ds_read_b64_tr_b16 v[54:55], %[b3] offset:768\n"
      "v_lshlrev_b32 %[t0], 16, v56\n"
      "v_and_b32 %[t1], %[msk], v56\n"
      "v_add_f32 %[c2], %[c2], %[t0]\n"
      "v_add_f32 %[c2], %[c2], %[t1]\n"
      "v_mfma_f32_32x32x16_bf16 v[80:95], v[60:63], v[196:199], v[80:95]\n"
      "ds_read_b64_tr_b16 v[56:57], %[b3] offset:64\n"
      "ds_read_b64_tr_b16 v[58:59], %[b3] offset:832\n"
      "v_lshlrev_b32 %[t0], 16, v61\n"
      "v_and_b32 %[t1], %[msk], v61\n"
      "v_add_f32 %[c0], %[c0], %[t0]\n"
      "v_add_f32 %[c0], %[c0], %[t1]\n"
      "s_waitcnt lgkmcnt(10)\n"
      "v_mfma_f32_32x32x16_bf16 v[96:111], v[64:67], v[196:199], v[96:111]\n"
      "ds_read_b64_tr_b16 v[60:61], %[b3] offset:128\n"
      "ds_read_b64_tr_b16 v[62:63], %[b3] offset:896\n"
      "v_lshlrev_b32 %[t0], 16, v65\n"
      "v_and_b32 %[t1], %[msk], v65\n"
      "v_add_f32 %[c1], %[c1], %[t0]\n"
      "v_add_f32 %[c1], %[c1], %[t1]\n"
      "v_mfma_f32_32x32x16_bf16 v[112:127], v[196:199], v[68:71], v[112:127]\n"
      "ds_read_b64_tr_b16 v[64:65], %[b0] offset:12288\n"
      "ds_read_b64_tr_b16 v[66:67], %[b0] offset:13056\n"
      "v_lshlrev_b32 %[t0], 16, v69\n"
      "v_and_b32 %[t1], %[msk], v69\n"
      "v_add_f32 %[c2], %[c2], %[t0]\n"
      "v_add_f32 %[c2], %[c2], %[t1]\n"
      "s_waitcnt lgkmcnt(10)\n"
      "v_mfma_f32_32x32x16_bf16 v[80:95], v[72:75], v[200:203], v[80:95]\n"
      "ds_read_b64_tr_b16 v[68:69], %[b0] offset:12352\n"
      "ds_read_b64_tr_b16 v[70:71], %[b0] offset:13120\n"
      "v_lshlrev_b32 %[t0], 16, v74\n"
      "v_and_b32 %[t1], %[msk], v74\n"
      "v_add_f32 %[c0], %[c0], %[t0]\n"
      "v_add_f32 %[c0], %[c0], %[t1]\n"
      "v_mfma_f32_32x32x16_bf16 v[96:111], v[76:79], v[200:203], v[96:111]\n"
      "ds_read_b64_tr_b16 v[72:73], %[b0] offset:12416\n"
      "ds_read_b64_tr_b16 v[74:75], %[b0] offset:13184\n"
      "v_lshlrev_b32 %[t0], 16, v78\n"
      "v_and_b32 %[t1], %[msk], v78\n"
      "v_add_f32 %[c1], %[c1], %[t0]\n"
      "v_add_f32 %[c1], %[c1], %[t1]\n"
      "s_waitcnt lgkmcnt(10)\n"
      "v_mfma_f32_32x32x16_bf16 v[112:127], v[200:203], v[48:51], v[112:127]\n"
      "ds_read_b64_tr_b16 v[76:77], %[b1] offset:12288\n"
      "ds_read_b64_tr_b16 v[78:79], %[b1] offset:13056\n"
      "v_lshlrev_b32 %[t0], 16, v50\n"
      "v_and_b32 %[t1], %[msk], v50\n"
      "v_add_f32 %[c2], %[c2], %[t0]\n"
      "v_add_f32 %[c2], %[c2], %[t1]\n"
      "v_mfma_f32_32x32x16_bf16 v[80:95], v[52:55], v[204:207], v[80:95]\n"
      "ds_read_b64_tr_b16 v[48:49], %[b1] offset:12352\n"
      "ds_read_b64_tr_b16 v[50:51], %[b1] offset:13120\n"
      "v_lshlrev_b32 %[t0], 16, v55\n"
      "v_and_b32 %[t1], %[msk], v55\n"
      "v_add_f32 %[c0], %[c0], %[t0]\n"
      "v_add_f32 %[c0], %[c0], %[t1]\n"
      "s_waitcnt lgkmcnt(10)\n"
      "v_mfma_f32_32x32x16_bf16 v[96:111], v[56:59], v[204:207], v[96:111]\n"
      "ds_read_b64_tr_b16 v[52:53], %[b1] offset:12416\n"
      "ds_read_b64_tr_b16 v[54:55], %[b1] offset:13184\n"
      "v_lshlrev_b32 %[t0], 16, v59\n"
      "v_and_b32 %[t1], %[msk], v59\n"
      "v_add_f32 %[c1], %[c1], %[t0]\n"
      "v_add_f32 %[c1], %[c1], %[t1]\n"
      "v_mfma_f32_32x32x16_bf16 v[112:127], v[204:207], v[60:63], v[112:127]\n"
      "ds_read_b64_tr_b16 v[56:57], %[b2] offset:12288\n"
      "ds_read_b64_tr_b16 v[58:59], %[b2] offset:13056\n"
      "v_lshlrev_b32 %[t0], 16, v63\n"
      "v_and_b32 %[t1], %[msk], v63\n"
      "v_add_f32 %[c2], %[c2], %[t0]\n"
      "v_add_f32 %[c2], %[c2], %[t1]\n"
      "s_waitcnt lgkmcnt(10)\n"
      "v_mfma_f32_32x32x16_bf16 v[80:95], v[64:67], v[208:211], v[80:95]\n"
      "ds_read_b64_tr_b16 v[60:61], %[b2] offset:12352\n"
      "ds_read_b64_tr_b16 v[62:63], %[b2] offset:13120\n"
      "v_lshlrev_b32 %[t0], 16, v64\n"
      "v_and_b32 %[t1], %[msk], v64\n"
      "v_add_f32 %[c0], %[c0], %[t0]\n"
      "v_add_f32 %[c0], %[c0], %[t1]\n"
      "v_mfma_f32_32x32x16_bf16 v[96:111], v[68:71], v[208:211], v[96:111]\n"
      "ds_read_b64_tr_b16 v[64:65], %[b2] offset:12416\n"
      "ds_read_b64_tr_b16 v[66:67], %[b2] offset:13184\n"
      "v_lshlrev_b32 %[t0], 16, v68\n"
      "v_and_b32 %[t1], %[msk], v68\n"
      "v_add_f32 %[c1], %[c1], %[t0]\n"
      "v_add_f32 %[c1], %[c1], %[t1]\n"
      "s_waitcnt lgkmcnt(10)\n"
      "v_mfma_f32_32x32x16_bf16 v[112:127], v[208:211], v[72:75], v[112:127]\n"
      "ds_read_b64_tr_b16 v[68:69], %[b3] offset:12288\n"
      "ds_read_b64_tr_b16 v[70:71], %[b3] offset:13056\n"
      "v_lshlrev_b32 %[t0], 16, v72\n"
      "v_and_b32 %[t1], %[msk], v72\n"
      "v_add_f32 %[c2], %[c2], %[t0]\n"
      "v_add_f32 %[c2], %[c2], %[t1]\n"
      "v_mfma_f32_32x32x16_bf16 v[80:95], v[76:79], v[212:215], v[80:95]\n"
      "ds_read_b64_tr_b16 v[72:73], %[b3] offset:12352\n"
      "ds_read_b64_tr_b16 v[74:75], %[b3] offset:13120\n"
      "v_lshlrev_b32 %[t0], 16, v77\n"
      "v_and_b32 %[t1], %[msk], v77\n"
      "v_add_f32 %[c0], %[c0], %[t0]\n"
      "v_add_f32 %[c0], %[c0], %[t1]\n"
      "s_waitcnt lgkmcnt(10)\n"
      "v_mfma_f32_32x32x16_bf16 v[96:111], v[48:51], v[212:215], v[96:111]\n"
      "ds_read_b64_tr_b16 v[76:77], %[b3] offset:12416\n"
      "ds_read_b64_tr_b16 v[78:79], %[b3] offset:13184\n"
      "v_lshlrev_b32 %[t0], 16, v49\n"
      "v_and_b32 %[t1], %[msk], v49\n"
      "v_add_f32 %[c1], %[c1], %[t0]\n"
      "v_add_f32 %[c1], %[c1], %[t1]\n"
      "v_mfma_f32_32x32x16_bf16 v[112:127], v[212:215], v[52:55], v[112:127]\n"
      "ds_read_b64_tr_b16 v[48:49], %[b0] offset:24576\n"
      "ds_read_b64_tr_b16 v[50:51], %[b0] offset:25344\n"
      "v_lshlrev_b32 %[t0], 16, v53\n"
      "v_and_b32 %[t1], %[msk], v53\n"
      "v_add_f32 %[c2], %[c2], %[t0]\n"
      "v_add_f32 %[c2], %[c2], %[t1]\n"
      "s_waitcnt lgkmcnt(10)\n"
      "v_mfma_f32_32x32x16_bf16 v[80:95], v[56:59], v[216:219], v[80:95]\n"
      "ds_read_b64_tr_b16 v[52:53], %[b0] offset:24640\n"
      "ds_read_b64_tr_b16 v[54:55], %[b0] offset:25408\n"
      "v_lshlrev_b32 %[t0], 16, v58\n"
      "v_and_b32 %[t1], %[msk], v58\n"
      "v_add_f32 %[c0], %[c0], %[t0]\n"
      "v_add_f32 %[c0], %[c0], %[t1]\n"
      "v_mfma_f32_32x32x16_bf16 v[96:111], v[60:63], v[216:219], v[96:111]\n"
      "ds_read_b64_tr_b16 v[56:57], %[b0] offset:24704\n"
      "ds_read_b64_tr_b16 v[58:59], %[b0] offset:25472\n"
      "v_lshlrev_b32 %[t0], 16, v62\n"
      "v_and_b32 %[t1], %[msk], v62\n"
      "v_add_f32 %[c1], %[c1], %[t0]\n"
      "v_add_f32 %[c1], %[c1], %[t1]\n"
      "s_waitcnt lgkmcnt(10)\n"
      "v_mfma_f32_32x32x16_bf16 v[112:127], v[216:219], v[64:67], v[112:127]\n"
      "ds_read_b64_tr_b16 v[60:61], %[b1] offset:24576\n"
      "ds_read_b64_tr_b16 v[62:63], %[b1] offset:25344\n"
      "v_lshlrev_b32 %[t0], 16, v66\n"
      "v_and_b32 %[t1], %[msk], v66\n"
      "v_add_f32 %[c2], %[c2], %[t0]\n"
      "v_add_f32 %[c2], %[c2], %[t1]\n"
      "v_mfma_f32_32x32x16_bf16 v[80:95], v[68:71], v[220:223], v[80:95]\n"
      "ds_read_b64_tr_b16 v[64:65], %[b1] offset:24640\n"
      "ds_read_b64_tr_b16 v[66:67], %[b1] offset:25408\n"
      "v_lshlrev_b32 %[t0], 16, v71\n"
      "v_and_b32 %[t1], %[msk], v71\n"
      "v_add_f32 %[c0], %[c0], %[t0]\n"
      "v_add_f32 %[c0], %[c0], %[t1]\n"
      "s_waitcnt lgkmcnt(10)\n"
      "v_mfma_f32_32x32x16_bf16 v[96:111], v[72:75], v[220:223], v[96:111]\n"
      "ds_read_b64_tr_b16 v[68:69], %[b1] offset:24704\n"
      "ds_read_b64_tr_b16 v[70:71], %[b1] offset:25472\n"
      "v_lshlrev_b32 %[t0], 16, v75\n"
      "v_and_b32 %[t1], %[msk], v75\n"
      "v_add_f32 %[c1], %[c1], %[t0]\n"
      "v_add_f32 %[c1], %[c1], %[t1]\n"
      "v_mfma_f32_32x32x16_bf16 v[112:127], v[220:223], v[76:79], v[112:127]\n"
      "ds_read_b64_tr_b16 v[72:73], %[b2] offset:24576\n"
      "ds_read_b64_tr_b16 v[74:75], %[b2] offset:25344\n"
      "v_lshlrev_b32 %[t0], 16, v79\n"
      "v_and_b32 %[t1], %[msk], v79\n"
      "v_add_f32 %[c2], %[c2], %[t0]\n"
      "v_add_f32 %[c2], %[c2], %[t1]\n"
      "s_waitcnt lgkmcnt(10)\n"
      "v_mfma_f32_32x32x16_bf16 v[80:95], v[48:51], v[224:227], v[80:95]\n"
      "ds_read_b64_tr_b16 v[76:77], %[b2] offset:24640\n"
      "ds_read_b64_tr_b16 v[78:79], %[b2] offset:25408\n"
      "v_lshlrev_b32 %[t0], 16, v48\n"
      "v_and_b32 %[t1], %[msk], v48\n"
      "v_add_f32 %[c0], %[c0], %[t0]\n"
      "v_add_f32 %[c0], %[c0], %[t1]\n"
      "v_mfma_f32_32x32x16_bf16 v[96:111], v[52:55], v[224:227], v[96:111]\n"
      "ds_read_b64_tr_b16 v[48:49], %[b2] offset:24704\n"
      "ds_read_b64_tr_b16 v[50:51], %[b2] offset:25472\n"
      "v_lshlrev_b32 %[t0], 16, v52\n"
      "v_and_b32 %[t1], %[msk], v52\n"
      "v_add_f32 %[c1], %[c1], %[t0]\n"
      "v_add_f32 %[c1], %[c1], %[t1]\n"
      "s_waitcnt lgkmcnt(10)\n"
      "v_mfma_f32_32x32x16_bf16 v[112:127], v[224:227], v[56:59], v[112:127]\n"
      "ds_read_b64_tr_b16 v[52:53], %[b3] offset:24576\n"
      "ds_read_b64_tr_b16 v[54:55], %[b3] offset:25344\n"
      "v_lshlrev_b32 %[t0], 16, v56\n"
      "v_and_b32 %[t1], %[msk], v56\n"
      "v_add_f32 %[c2], %[c2], %[t0]\n"
      "v_add_f32 %[c2], %[c2], %[t1]\n"
      "v_mfma_f32_32x32x16_bf16 v[80:95], v[60:63], v[228:231], v[80:95]\n"
      "ds_read_b64_tr_b16 v[56:57], %[b3] offset:24640\n"
      "ds_read_b64_tr_b16 v[58:59], %[b3] offset:25408\n"
      "v_lshlrev_b32 %[t0], 16, v61\n"
      "v_and_b32 %[t1], %[msk], v61\n"
      "v_add_f32 %[c0], %[c0], %[t0]\n"
      "v_add_f32 %[c0], %[c0], %[t1]\n"
      "s_waitcnt lgkmcnt(10)\n"
      "v_mfma_f32_32x32x16_bf16 v[96:111], v[64:67], v[228:231], v[96:111]\n"
      "ds_read_b64_tr_b16 v[60:61], %[b3] offset:24704\n"
      "ds_read_b64_tr_b16 v[62:63], %[b3] offset:25472\n"
      "v_lshlrev_b32 %[t0], 16, v65\n"
      "v_and_b32 %[t1], %[msk], v65\n"
      "v_add_f32 %[c1], %[c1], %[t0]\n"
      "v_add_f32 %[c1], %[c1], %[t1]\n"
      "v_mfma_f32_32x32x16_bf16 v[112:127], v[228:231], v[68:71], v[112:127]\n"
      "ds_read_b64_tr_b16 v[64:65], %[b0] offset:36864\n"
      "ds_read_b64_tr_b16 v[66:67], %[b0] offset:37632\n"
      "v_lshlrev_b32 %[t0], 16, v69\n"
      "v_and_b32 %[t1], %[msk], v69\n"
      "v_add_f32 %[c2], %[c2], %[t0]\n"
      "v_add_f32 %[c2], %[c2], %[t1]\n"
      "s_waitcnt lgkmcnt(10)\n"
      "v_mfma_f32_32x32x16_bf16 v[80:95], v[72:75], v[232:235], v[80:95]\n"
      "ds_read_b64_tr_b16 v[68:69], %[b0] offset:36928\n"
      "ds_read_b64_tr_b16 v[70:71], %[b0] offset:37696\n"
      "v_lshlrev_b32 %[t0], 16, v74\n"
      "v_and_b32 %[t1], %[msk], v74\n"
      "v_add_f32 %[c0], %[c0], %[t0]\n"
      "v_add_f32 %[c0], %[c0], %[t1]\n"
      "v_mfma_f32_32x32x16_bf16 v[96:111], v[76:79], v[232:235], v[96:111]\n"
      "ds_read_b64_tr_b16 v[72:73], %[b0] offset:36992\n"
      "ds_read_b64_tr_b16 v[74:75], %[b0] offset:37760\n"
      "v_lshlrev_b32 %[t0], 16, v78\n"
      "v_and_b32 %[t1], %[msk], v78\n"
      "v_add_f32 %[c1], %[c1], %[t0]\n"
      "v_add_f32 %[c1], %[c1], %[t1]\n"
      "s_waitcnt lgkmcnt(10)\n"
      "v_mfma_f32_32x32x16_bf16 v[112:127], v[232:235], v[48:51], v[112:127]\n"
      "ds_read_b64_tr_b16 v[76:77], %[b1] offset:36864\n"
      "ds_read_b64_tr_b16 v[78:79], %[b1] offset:37632\n"
      "v_lshlrev_b32 %[t0], 16, v50\n"
      "v_and_b32 %[t1], %[msk], v50\n"
      "v_add_f32 %[c2], %[c2], %[t0]\n"
      "v_add_f32 %[c2], %[c2], %[t1]\n"
      "v_mfma_f32_32x32x16_bf16 v[80:95], v[52:55], v[236:239], v[80:95]\n"
      "ds_read_b64_tr_b16 v[48:49], %[b1] offset:36928\n"
      "ds_read_b64_tr_b16 v[50:51], %[b1] offset:37696\n"
      "v_lshlrev_b32 %[t0], 16, v55\n"
      "v_and_b32 %[t1], %[msk], v55\n"
      "v_add_f32 %[c0], %[c0], %[t0]\n"
      "v_add_f32 %[c0], %[c0], %[t1]\n"
      "s_waitcnt lgkmcnt(10)\n"
      "v_mfma_f32_32x32x16_bf16 v[96:111], v[56:59], v[236:239], v[96:111]\n"
      "ds_read_b64_tr_b16 v[52:53], %[b1] offset:36992\n"
      "ds_read_b64_tr_b16 v[54:55], %[b1] offset:37760\n"
      "v_lshlrev_b32 %[t0], 16, v59\n"
      "v_and_b32 %[t1], %[msk], v59\n"
      "v_add_f32 %[c1], %[c1], %[t0]\n"
      "v_add_f32 %[c1], %[c1], %[t1]\n"
      "v_mfma_f32_32x32x16_bf16 v[112:127], v[236:239], v[60:63], v[112:127]\n"
      "ds_read_b64_tr_b16 v[56:57], %[b2] offset:36864\n"
      "ds_read_b64_tr_b16 v[58:59], %[b2] offset:37632\n"
      "v_lshlrev_b32 %[t0], 16, v63\n"
      "v_and_b32 %[t1], %[msk], v63\n"
      "v_add_f32 %[c2], %[c2], %[t0]\n"
      "v_add_f32 %[c2], %[c2], %[t1]\n"
      "s_waitcnt lgkmcnt(10)\n"
      "v_mfma_f32_32x32x16_bf16 v[80:95], v[64:67], v[240:243], v[80:95]\n"
      "ds_read_b64_tr_b16 v[60:61], %[b2] offset:36928\n"
      "ds_read_b64_tr_b16 v[62:63], %[b2] offset:37696\n"
      "v_lshlrev_b32 %[t0], 16, v64\n"
      "v_and_b32 %[t1], %[msk], v64\n"
      "v_add_f32 %[c0], %[c0], %[t0]\n"
      "v_add_f32 %[c0], %[c0], %[t1]\n"
      "v_mfma_f32_32x32x16_bf16 v[96:111], v[68:71], v[240:243], v[96:111]\n"
      "ds_read_b64_tr_b16 v[64:65], %[b2] offset:36992\n"
      "ds_read_b64_tr_b16 v[66:67], %[b2] offset:37760\n"
      "v_lshlrev_b32 %[t0], 16, v68\n"
      "v_and_b32 %[t1], %[msk], v68\n"
      "v_add_f32 %[c1], %[c1], %[t0]\n"
      "v_add_f32 %[c1], %[c1], %[t1]\n"
      "s_waitcnt lgkmcnt(10)\n"
      "v_mfma_f32_32x32x16_bf16 v[112:127], v[240:243], v[72:75], v[112:127]\n"
      "ds_read_b64_tr_b16 v[68:69], %[b3] offset:36864\n"
      "ds_read_b64_tr_b16 v[70:71], %[b3] offset:37632\n"
      "v_lshlrev_b32 %[t0], 16, v72\n"
      "v_and_b32 %[t1], %[msk], v72\n"
      "v_add_f32 %[c2], %[c2], %[t0]\n"
      "v_add_f32 %[c2], %[c2], %[t1]\n"
      "v_mfma_f32_32x32x16_bf16 v[80:95], v[76:79], v[244:247], v[80:95]\n"
      "ds_read_b64_tr_b16 v[72:73], %[b3] offset:36928\n"
      "ds_read_b64_tr_b16 v[74:75], %[b3] offset:37696\n"
      "v_lshlrev_b32 %[t0], 16, v77\n"
      "v_and_b32 %[t1], %[msk], v77\n"
      "v_add_f32 %[c0], %[c0], %[t0]\n"
      "v_add_f32 %[c0], %[c0], %[t1]\n"
      "s_waitcnt lgkmcnt(10)\n"
      "v_mfma_f32_32x32x16_bf16 v[96:111], v[48:51], v[244:247], v[96:111]\n"
      "ds_read_b64_tr_b16 v[76:77], %[b3] offset:36992\n"
      "ds_read_b64_tr_b16 v[78:79], %[b3] offset:37760\n"
      "v_lshlrev_b32 %[t0], 16, v49\n"
      "v_and_b32 %[t1], %[msk], v49\n"
      "v_add_f32 %[c1], %[c1], %[t0]\n"
      "v_add_f32 %[c1], %[c1], %[t1]\n"
      "v_mfma_f32_32x32x16_bf16 v[112:127], v[244:247], v[52:55], v[112:127]\n"
      "v_lshlrev_b32 %[t0], 16, v53\n"
      "v_and_b32 %[t1], %[msk], v53\n"
      "v_add_f32 %[c2], %[c2], %[t0]\n"
      "v_add_f32 %[c2], %[c2], %[t1]\n"
      "s_waitcnt lgkmcnt(8)\n"
      "v_mfma_f32_32x32x16_bf16 v[80:95], v[56:59], v[248:251], v[80:95]\n"
      "v_lshlrev_b32 %[t0], 16, v58\n"
      "v_and_b32 %[t1], %[msk], v58\n"
      "v_add_f32 %[c0], %[c0], %[t0]\n"
      "v_add_f32 %[c0], %[c0], %[t1]\n"
      "v_mfma_f32_32x32x16_bf16 v[96:111], v[60:63], v[248:251], v[96:111]\n"
      "v_lshlrev_b32 %[t0], 16, v62\n"
      "v_and_b32 %[t1], %[msk], v62\n"
      "v_add_f32 %[c1], %[c1], %[t0]\n"
      "v_add_f32 %[c1], %[c1], %[t1]\n"
      "s_waitcnt lgkmcnt(4)\n"
      "v_mfma_f32_32x32x16_bf16 v[112:127], v[248:251], v[64:67], v[112:127]\n"
      "v_lshlrev_b32 %[t0], 16, v66\n"
      "v_and_b32 %[t1], %[msk], v66\n"
      "v_add_f32 %[c2], %[c2], %[t0]\n"
      "v_add_f32 %[c2], %[c2], %[t1]\n"
      "v_mfma_f32_32x32x16_bf16 v[80:95], v[68:71], v[252:255], v[80:95]\n"
      "v_lshlrev_b32 %[t0], 16, v71\n"
      "v_and_b32 %[t1], %[msk], v71\n"
      "v_add_f32 %[c0], %[c0], %[t0]\n"
      "v_add_f32 %[c0], %[c0], %[t1]\n"
      "s_waitcnt lgkmcnt(0)\n"
      "v_mfma_f32_32x32x16_bf16 v[96:111], v[72:75], v[252:255], v[96:111]\n"
      "v_lshlrev_b32 %[t0], 16, v75\n"
      "v_and_b32 %[t1], %[msk], v75\n"
      "v_add_f32 %[c1], %[c1], %[t0]\n"
      "v_add_f32 %[c1], %[c1], %[t1]\n"
      "v_mfma_f32_32x32x16_bf16 v[112:127], v[252:255], v[76:79], v[112:127]\n"
      "v_lshlrev_b32 %[t0], 16, v79\n"
      "v_and_b32 %[t1], %[msk], v79\n"
      "v_add_f32 %[c2], %[c2], %[t0]\n"
      "v_add_f32 %[c2], %[c2], %[t1]\n"
      "s_nop 15\n"
      "s_branch 3f\n"
      "2:\n"
      "ds_read_b64_tr_b16 v[48:49], %[b0] offset:0\n"
      "ds_read_b64_tr_b16 v[50:51], %[b0] offset:768\n"
      "ds_read_b64_tr_b16 v[52:53], %[b0] offset:64\n"
      "ds_read_b64_tr_b16 v[54:55], %[b0] offset:832\n"
      "ds_read_b64_tr_b16 v[56:57], %[b0] offset:128\n"
      "ds_read_b64_tr_b16 v[58:59], %[b0] offset:896\n"
      "ds_read_b64_tr_b16 v[60:61], %[b1] offset:0\n"
      "ds_read_b64_tr_b16 v[62:63], %[b1] offset:768\n"
      "ds_read_b64_tr_b16 v[64:65], %[b1] offset:64\n"
      "ds_read_b64_tr_b16 v[66:67], %[b1] offset:832\n"
      "ds_read_b64_tr_b16 v[68:69], %[b1] offset:128\n"
      "ds_read_b64_tr_b16 v[70:71], %[b1] offset:896\n"
      "ds_read_b64_tr_b16 v[72:73], %[b2] offset:0\n"
      "ds_read_b64_tr_b16 v[74:75], %[b2] offset:768\n"
      "s_waitcnt lgkmcnt(10)\n"
      "v_mfma_f32_32x32x16_bf16 v[80:95], v[192:195], v[48:51], v[80:95]\n"
      "ds_read_b64_tr_b16 v[76:77], %[b2] offset:64\n"
      "ds_read_b64_tr_b16 v[78:79], %[b2] offset:832\n"
      "v_lshlrev_b32 %[t0], 16, v48\n"
      "v_and_b32 %[t1], %[msk], v48\n"
      "v_add_f32 %[c0], %[c0], %[t0]\n"
      "v_add_f32 %[c0], %[c0], %[t1]\n"
      "v_mfma_f32_32x32x16_bf16 v[96:111], v[192:195], v[52:55], v[96:111]\n"
      "ds_read_b64_tr_b16 v[48:49], %[b2] offset:128\n"
      "ds_read_b64_tr_b16 v[50:51], %[b2] offset:896\n"
      "v_lshlrev_b32 %[t0], 16, v52\n"
      "v_and_b32 %[t1], %[msk], v52\n"
      "v_add_f32 %[c1], %[c1], %[t0]\n"
      "v_add_f32 %[c1], %[c1], %[t1]\n"
      "s_waitcnt lgkmcnt(10)\n"
      "v_mfma_f32_32x32x16_bf16 v[112:127], v[192:195], v[56:59], v[112:127]\n"
      "ds_read_b64_tr_b16 v[52:53], %[b3] offset:0\n"
      "ds_read_b64_tr_b16 v[54:55], %[b3] offset:768\n"
      "v_lshlrev_b32 %[t0], 16, v56\n"
      "v_and_b32 %[t1], %[msk], v56\n"
      "v_add_f32 %[c2], %[c2], %[t0]\n"
      "v_add_f32 %[c2], %[c2], %[t1]\n"
      "v_mfma_f32_32x32x16_bf16 v[80:95], v[196:199], v[60:63], v[80:95]\n"
      "ds_read_b64_tr_b16 v[56:57], %[b3] offset:64\n"
      "ds_read_b64_tr_b16 v[58:59], %[b3] offset:832\n"
      "v_lshlrev_b32 %[t0], 16, v61\n"
      "v_and_b32 %[t1], %[msk], v61\n"
      "v_add_f32 %[c0], %[c0], %[t0]\n"
      "v_add_f32 %[c0], %[c0], %[t1]\n"
      "s_waitcnt lgkmcnt(10)\n"
      "v_mfma_f32_32x32x16_bf16 v[96:111], v[196:199], v[64:67], v[96:111]\n"
      "ds_read_b64_tr_b16 v[60:61], %[b3] offset:128\n"
      "ds_read_b64_tr_b16 v[62:63], %[b3] offset:896\n"
      "v_lshlrev_b32 %[t0], 16, v65\n"
      "v_and_b32 %[t1], %[msk], v65\n"
      "v_add_f32 %[c1], %[c1], %[t0]\n"
      "v_add_f32 %[c1], %[c1], %[t1]\n"
      "v_mfma_f32_32x32x16_bf16 v[112:127], v[196:199], v[68:71], v[112:127]\n"
      "ds_read_b64_tr_b16 v[64:65], %[b0] offset:12288\n"
      "ds_read_b64_tr_b16 v[66:67], %[b0] offset:13056\n"
      "v_lshlrev_b32 %[t0], 16, v69\n"
      "v_and_b32 %[t1], %[msk], v69\n"
      "v_add_f32 %[c2], %[c2], %[t0]\n"
      "v_add_f32 %[c2], %[c2], %[t1]\n"
      "s_waitcnt lgkmcnt(10)\n"
      "v_mfma_f32_32x32x16_bf16 v[80:95], v[200:203], v[72:75], v[80:95]\n"
      "ds_read_b64_tr_b16 v[68:69], %[b0] offset:12352\n"
      "ds_read_b64_tr_b16 v[70:71], %[b0] offset:13120\n"
      "v_lshlrev_b32 %[t0], 16, v74\n"
      "v_and_b32 %[t1], %[msk], v74\n"
      "v_add_f32 %[c0], %[c0], %[t0]\n"
      "v_add_f32 %[c0], %[c0], %[t1]\n"
      "v_mfma_f32_32x32x16_bf16 v[96:111], v[200:203], v[76:79], v[96:111]\n"
      "ds_read_b64_tr_b16 v[72:73], %[b0] offset:12416\n"
      "ds_read_b64_tr_b16 v[74:75], %[b0] offset:13184\n"
      "v_lshlrev_b32 %[t0], 16, v78\n"
      "v_and_b32 %[t1], %[msk], v78\n"
      "v_add_f32 %[c1], %[c1], %[t0]\n"
      "v_add_f32 %[c1], %[c1], %[t1]\n"
      "s_waitcnt lgkmcnt(10)\n"
      "v_mfma_f32_32x32x16_bf16 v[112:127], v[200:203], v[48:51], v[112:127]\n"
      "ds_read_b64_tr_b16 v[76:77], %[b1] offset:12288\n"
      "ds_read_b64_tr_b16 v[78:79], %[b1] offset:13056\n"
      "v_lshlrev_b32 %[t0], 16, v50\n"
      "v_and_b32 %[t1], %[msk], v50\n"
      "v_add_f32 %[c2], %[c2], %[t0]\n"
      "v_add_f32 %[c2], %[c2], %[t1]\n"
      "v_mfma_f32_32x32x16_bf16 v[80:95], v[204:207], v[52:55], v[80:95]\n"
      "ds_read_b64_tr_b16 v[48:49], %[b1] offset:12352\n"
      "ds_read_b64_tr_b16 v[50:51], %[b1] offset:13120\n"
      "v_lshlrev_b32 %[t0], 16, v55\n"
      "v_and_b32 %[t1], %[msk], v55\n"
      "v_add_f32 %[c0], %[c0], %[t0]\n"
      "v_add_f32 %[c0], %[c0], %[t1]\n"
      "s_waitcnt lgkmcnt(10)\n"
      "v_mfma_f32_32x32x16_bf16 v[96:111], v[204:207], v[56:59], v[96:111]\n"
      "ds_read_b64_tr_b16 v[52:53], %[b1] offset:12416\n"
      "ds_read_b64_tr_b16 v[54:55], %[b1] offset:13184\n"
      "v_lshlrev_b32 %[t0], 16, v59\n"
      "v_and_b32 %[t1], %[msk], v59\n"
      "v_add_f32 %[c1], %[c1], %[t0]\n"
      "v_add_f32 %[c1], %[c1], %[t1]\n"
      "v_mfma_f32_32x32x16_bf16 v[112:127], v[204:207], v[60:63], v[112:127]\n"
      "ds_read_b64_tr_b16 v[56:57], %[b2] offset:12288\n"
      "ds_read_b64_tr_b16 v[58:59], %[b2] offset:13056\n"
      "v_lshlrev_b32 %[t0], 16, v63\n"
      "v_and_b32 %[t1], %[msk], v63\n"
      "v_add_f32 %[c2], %[c2], %[t0]\n"
      "v_add_f32 %[c2], %[c2], %[t1]\n"
      "s_waitcnt lgkmcnt(10)\n"
      "v_mfma_f32_32x32x16_bf16 v[80:95], v[208:211], v[64:67], v[80:95]\n"
      "ds_read_b64_tr_b16 v[60:61], %[b2] offset:12352\n"
      "ds_read_b64_tr_b16 v[62:63], %[b2] offset:13120\n"
      "v_lshlrev_b32 %[t0], 16, v64\n"
      "v_and_b32 %[t1], %[msk], v64\n"
      "v_add_f32 %[c0], %[c0], %[t0]\n"
      "v_add_f32 %[c0], %[c0], %[t1]\n"
      "v_mfma_f32_32x32x16_bf16 v[96:111], v[208:211], v[68:71], v[96:111]\n"
      "ds_read_b64_tr_b16 v[64:65], %[b2] offset:12416\n"
      "ds_read_b64_tr_b16 v[66:67], %[b2] offset:13184\n"
      "v_lshlrev_b32 %[t0], 16, v68\n"
      "v_and_b32 %[t1], %[msk], v68\n"
      "v_add_f32 %[c1], %[c1], %[t0]\n"
      "v_add_f32 %[c1], %[c1], %[t1]\n"
      "s_waitcnt lgkmcnt(10)\n"
      "v_mfma_f32_32x32x16_bf16 v[112:127], v[208:211], v[72:75], v[112:127]\n"
      "ds_read_b64_tr_b16 v[68:69], %[b3] offset:12288\n"
      "ds_read_b64_tr_b16 v[70:71], %[b3] offset:13056\n"
      "v_lshlrev_b32 %[t0], 16, v72\n"
      "v_and_b32 %[t1], %[msk], v72\n"
      "v_add_f32 %[c2], %[c2], %[t0]\n"
      "v_add_f32 %[c2], %[c2], %[t1]\n"
      "v_mfma_f32_32x32x16_bf16 v[80:95], v[212:215], v[76:79], v[80:95]\n"
      "ds_read_b64_tr_b16 v[72:73], %[b3] offset:12352\n"
      "ds_read_b64_tr_b16 v[74:75], %[b3] offset:13120\n"
      "v_lshlrev_b32 %[t0], 16, v77\n"
      "v_and_b32 %[t1], %[msk], v77\n"
      "v_add_f32 %[c0], %[c0], %[t0]\n"
      "v_add_f32 %[c0], %[c0], %[t1]\n"
      "s_waitcnt lgkmcnt(10)\n"
      "v_mfma_f32_32x32x16_bf16 v[96:111], v[212:215], v[48:51], v[96:111]\n"
      "ds_read_b64_tr_b16 v[76:77], %[b3] offset:12416\n"
      "ds_read_b64_tr_b16 v[78:79], %[b3] offset:13184\n"
      "v_lshlrev_b32 %[t0], 16, v49\n"
      "v_and_b32 %[t1], %[msk], v49\n"
      "v_add_f32 %[c1], %[c1], %[t0]\n"
      "v_add_f32 %[c1], %[c1], %[t1]\n"
      "v_mfma_f32_32x32x16_bf16 v[112:127], v[212:215], v[52:55], v[112:127]\n"
      "ds_read_b64_tr_b16 v[48:49], %[b0] offset:24576\n"
      "ds_read_b64_tr_b16 v[50:51], %[b0] offset:25344\n"
      "v_lshlrev_b32 %[t0], 16, v53\n"
      "v_and_b32 %[t1], %[msk], v53\n"
      "v_add_f32 %[c2], %[c2], %[t0]\n"
      "v_add_f32 %[c2], %[c2], %[t1]\n"
      "s_waitcnt lgkmcnt(10)\n"
      "v_mfma_f32_32x32x16_bf16 v[80:95], v[216:219], v[56:59], v[80:95]\n"
      "ds_read_b64_tr_b16 v[52:53], %[b0] offset:24640\n"
      "ds_read_b64_tr_b16 v[54:55], %[b0] offset:25408\n"
      "v_lshlrev_b32 %[t0], 16, v58\n"
      "v_and_b32 %[t1], %[msk], v58\n"
      "v_add_f32 %[c0], %[c0], %[t0]\n"
      "v_add_f32 %[c0], %[c0], %[t1]\n"
      "v_mfma_f32_32x32x16_bf16 v[96:111], v[216:219], v[60:63], v[96:111]\n"
      "ds_read_b64_tr_b16 v[56:57], %[b0] offset:24704\n"
      "ds_read_b64_tr_b16 v[58:59], %[b0] offset:25472\n"
      "v_lshlrev_b32 %[t0], 16, v62\n"
      "v_and_b32 %[t1], %[msk], v62\n"
      "v_add_f32 %[c1], %[c1], %[t0]\n"
      "v_add_f32 %[c1], %[c1], %[t1]\n"
      "s_waitcnt lgkmcnt(10)\n"
      "v_mfma_f32_32x32x16_bf16 v[112:127], v[216:219], v[64:67], v[112:127]\n"
      "ds_read_b64_tr_b16 v[60:61], %[b1] offset:24576\n"
      "ds_read_b64_tr_b16 v[62:63], %[b1] offset:25344\n"
      "v_lshlrev_b32 %[t0], 16, v66\n"
      "v_and_b32 %[t1], %[msk], v66\n"
      "v_add_f32 %[c2], %[c2], %[t0]\n"
      "v_add_f32 %[c2], %[c2], %[t1]\n"
      "v_mfma_f32_32x32x16_bf16 v[80:95], v[220:223], v[68:71], v[80:95]\n"
      "ds_read_b64_tr_b16 v[64:65], %[b1] offset:24640\n"
      "ds_read_b64_tr_b16 v[66:67], %[b1] offset:25408\n"
      "v_lshlrev_b32 %[t0], 16, v71\n"
      "v_and_b32 %[t1], %[msk], v71\n"
      "v_add_f32 %[c0], %[c0], %[t0]\n"
      "v_add_f32 %[c0], %[c0], %[t1]\n"
      "s_waitcnt lgkmcnt(10)\n"
      "v_mfma_f32_32x32x16_bf16 v[96:111], v[220:223], v[72:75], v[96:111]\n"
      "ds_read_b64_tr_b16 v[68:69], %[b1] offset:24704\n"
      "ds_read_b64_tr_b16 v[70:71], %[b1] offset:25472\n"
      "v_lshlrev_b32 %[t0], 16, v75\n"
      "v_and_b32 %[t1], %[msk], v75\n"
      "v_add_f32 %[c1], %[c1], %[t0]\n"
      "v_add_f32 %[c1], %[c1], %[t1]\n"
      "v_mfma_f32_32x32x16_bf16 v[112:127], v[220:223], v[76:79], v[112:127]\n"
      "ds_read_b64_tr_b16 v[72:73], %[b2] offset:24576\n"
      "ds_read_b64_tr_b16 v[74:75], %[b2] offset:25344\n"
      "v_lshlrev_b32 %[t0], 16, v79\n"
      "v_and_b32 %[t1], %[msk], v79\n"
      "v_add_f32 %[c2], %[c2], %[t0]\n"
      "v_add_f32 %[c2], %[c2], %[t1]\n"
      "s_waitcnt lgkmcnt(10)\n"
      "v_mfma_f32_32x32x16_bf16 v[80:95], v[224:227], v[48:51], v[80:95]\n"
      "ds_read_b64_tr_b16 v[76:77], %[b2] offset:24640\n"
      "ds_read_b64_tr_b16 v[78:79], %[b2] offset:25408\n"
      "v_lshlrev_b32 %[t0], 16, v48\n"
      "v_and_b32 %[t1], %[msk], v48\n"
      "v_add_f32 %[c0], %[c0], %[t0]\n"
      "v_add_f32 %[c0], %[c0], %[t1]\n"
      "v_mfma_f32_32x32x16_bf16 v[96:111], v[224:227], v[52:55], v[96:111]\n"
      "ds_read_b64_tr_b16 v[48:49], %[b2] offset:24704\n"
      "ds_read_b64_tr_b16 v[50:51], %[b2] offset:25472\n"
      "v_lshlrev_b32 %[t0], 16, v52\n"
      "v_and_b32 %[t1], %[msk], v52\n"
      "v_add_f32 %[c1], %[c1], %[t0]\n"
      "v_add_f32 %[c1], %[c1], %[t1]\n"
      "s_waitcnt lgkmcnt(10)\n"
      "v_mfma_f32_32x32x16_bf16 v[112:127], v[224:227], v[56:59], v[112:127]\n"
      "ds_read_b64_tr_b16 v[52:53], %[b3] offset:24576\n"
      "ds_read_b64_tr_b16 v[54:55], %[b3] offset:25344\n"
      "v_lshlrev_b32 %[t0], 16, v56\n"
      "v_and_b32 %[t1], %[msk], v56\n"
      "v_add_f32 %[c2], %[c2], %[t0]\n"
      "v_add_f32 %[c2], %[c2], %[t1]\n"
      "v_mfma_f32_32x32x16_bf16 v[80:95], v[228:231], v[60:63], v[80:95]\n"
      "ds_read_b64_tr_b16 v[56:57], %[b3] offset:24640\n"
      "ds_read_b64_tr_b16 v[58:59], %[b3] offset:25408\n"
      "v_lshlrev_b32 %[t0], 16, v61\n"
      "v_and_b32 %[t1], %[msk], v61\n"
      "v_add_f32 %[c0], %[c0], %[t0]\n"
      "v_add_f32 %[c0], %[c0], %[t1]\n"
      "s_waitcnt lgkmcnt(10)\n"
      "v_mfma_f32_32x32x16_bf16 v[96:111], v[228:231], v[64:67], v[96:111]\n"
      "ds_read_b64_tr_b16 v[60:61], %[b3] offset:24704\n"
      "ds_read_b64_tr_b16 v[62:63], %[b3] offset:25472\n"
      "v_lshlrev_b32 %[t0], 16, v65\n"
      "v_and_b32 %[t1], %[msk], v65\n"
      "v_add_f32 %[c1], %[c1], %[t0]\n"
      "v_add_f32 %[c1], %[c1], %[t1]\n"
      "v_mfma_f32_32x32x16_bf16 v[112:127], v[228:231], v[68:71], v[112:127]\n"
      "ds_read_b64_tr_b16 v[64:65], %[b0] offset:36864\n"
      "ds_read_b64_tr_b16 v[66:67], %[b0] offset:37632\n"
      "v_lshlrev_b32 %[t0], 16, v69\n"
      "v_and_b32 %[t1], %[msk], v69\n"
      "v_add_f32 %[c2], %[c2], %[t0]\n"
      "v_add_f32 %[c2], %[c2], %[t1]\n"
      "s_waitcnt lgkmcnt(10)\n"
      "v_mfma_f32_32x32x16_bf16 v[80:95], v[232:235], v[72:75], v[80:95]\n"
      "ds_read_b64_tr_b16 v[68:69], %[b0] offset:36928\n"
      "ds_read_b64_tr_b16 v[70:71], %[b0] offset:37696\n"
      "v_lshlrev_b32 %[t0], 16, v74\n"
      "v_and_b32 %[t1], %[msk], v74\n"
      "v_add_f32 %[c0], %[c0], %[t0]\n"
      "v_add_f32 %[c0], %[c0], %[t1]\n"
      "v_mfma_f32_32x32x16_bf16 v[96:111], v[232:235], v[76:79], v[96:111]\n"
      "ds_read_b64_tr_b16 v[72:73], %[b0] offset:36992\n"
      "ds_read_b64_tr_b16 v[74:75], %[b0] offset:37760\n"
      "v_lshlrev_b32 %[t0], 16, v78\n"
      "v_and_b32 %[t1], %[msk], v78\n"
      "v_add_f32 %[c1], %[c1], %[t0]\n"
      "v_add_f32 %[c1], %[c1], %[t1]\n"
      "s_waitcnt lgkmcnt(10)\n"
      "v_mfma_f32_32x32x16_bf16 v[112:127], v[232:235], v[48:51], v[112:127]\n"
      "ds_read_b64_tr_b16 v[76:77], %[b1] offset:36864\n"
      "ds_read_b64_tr_b16 v[78:79], %[b1] offset:37632\n"
      "v_lshlrev_b32 %[t0], 16, v50\n"
      "v_and_b32 %[t1], %[msk], v50\n"
      "v_add_f32 %[c2], %[c2], %[t0]\n"
      "v_add_f32 %[c2], %[c2], %[t1]\n"
      "v_mfma_f32_32x32x16_bf16 v[80:95], v[236:239], v[52:55], v[80:95]\n"
      "ds_read_b64_tr_b16 v[48:49], %[b1] offset:36928\n"
      "ds_read_b64_tr_b16 v[50:51], %[b1] offset:37696\n"
      "v_lshlrev_b32 %[t0], 16, v55\n"
      "v_and_b32 %[t1], %[msk], v55\n"
      "v_add_f32 %[c0], %[c0], %[t0]\n"
      "v_add_f32 %[c0], %[c0], %[t1]\n"
      "s_waitcnt lgkmcnt(10)\n"
      "v_mfma_f32_32x32x16_bf16 v[96:111], v[236:239], v[56:59], v[96:111]\n"
      "ds_read_b64_tr_b16 v[52:53], %[b1] offset:36992\n"
      "ds_read_b64_tr_b16 v[54:55], %[b1] offset:37760\n"
      "v_lshlrev_b32 %[t0], 16, v59\n"
      "v_and_b32 %[t1], %[msk], v59\n"
      "v_add_f32 %[c1], %[c1], %[t0]\n"
      "v_add_f32 %[c1], %[c1], %[t1]\n"
      "v_mfma_f32_32x32x16_bf16 v[112:127], v[236:239], v[60:63], v[112:127]\n"
      "ds_read_b64_tr_b16 v[56:57], %[b2] offset:36864\n"
      "ds_read_b64_tr_b16 v[58:59], %[b2] offset:37632\n"
      "v_lshlrev_b32 %[t0], 16, v63\n"
      "v_and_b32 %[t1], %[msk], v63\n"
      "v_add_f32 %[c2], %[c2], %[t0]\n"
      "v_add_f32 %[c2], %[c2], %[t1]\n"
      "s_waitcnt lgkmcnt(10)\n"
      "v_mfma_f32_32x32x16_bf16 v[80:95], v[240:243], v[64:67], v[80:95]\n"
      "ds_read_b64_tr_b16 v[60:61], %[b2] offset:36928\n"
      "ds_read_b64_tr_b16 v[62:63], %[b2] offset:37696\n"
      "v_lshlrev_b32 %[t0], 16, v64\n"
      "v_and_b32 %[t1], %[msk], v64\n"
      "v_add_f32 %[c0], %[c0], %[t0]\n"
      "v_add_f32 %[c0], %[c0], %[t1]\n"
      "v_mfma_f32_32x32x16_bf16 v[96:111], v[240:243], v[68:71], v[96:111]\n"
      "ds_read_b64_tr_b16 v[64:65], %[b2] offset:36992\n"
      "ds_read_b64_tr_b16 v[66:67], %[b2] offset:37760\n"
      "v_lshlrev_b32 %[t0], 16, v68\n"
      "v_and_b32 %[t1], %[msk], v68\n"
      "v_add_f32 %[c1], %[c1], %[t0]\n"
      "v_add_f32 %[c1], %[c1], %[t1]\n"
      "s_waitcnt lgkmcnt(10)\n"
      "v_mfma_f32_32x32x16_bf16 v[112:127], v[240:243], v[72:75], v[112:127]\n"
      "ds_read_b64_tr_b16 v[68:69], %[b3] offset:36864\n"
      "ds_read_b64_tr_b16 v[70:71], %[b3] offset:37632\n"
      "v_lshlrev_b32 %[t0], 16, v72\n"
      "v_and_b32 %[t1], %[msk], v72\n"
      "v_add_f32 %[c2], %[c2], %[t0]\n"
      "v_add_f32 %[c2], %[c2], %[t1]\n"
      "v_mfma_f32_32x32x16_bf16 v[80:95], v[244:247], v[76:79], v[80:95]\n"
      "ds_read_b64_tr_b16 v[72:73], %[b3] offset:36928\n"
      "ds_read_b64_tr_b16 v[74:75], %[b3] offset:37696\n"
      "v_lshlrev_b32 %[t0], 16, v77\n"
      "v_and_b32 %[t1], %[msk], v77\n"
      "v_add_f32 %[c0], %[c0], %[t0]\n"
      "v_add_f32 %[c0], %[c0], %[t1]\n"
      "s_waitcnt lgkmcnt(10)\n"
      "v_mfma_f32_32x32x16_bf16 v[96:111], v[244:247], v[48:51], v[96:111]\n"
      "ds_read_b64_tr_b16 v[76:77], %[b3] offset:36992\n"
      "ds_read_b64_tr_b16 v[78:79], %[b3] offset:37760\n"
      "v_lshlrev_b32 %[t0], 16, v49\n"
      "v_and_b32 %[t1], %[msk], v49\n"
      "v_add_f32 %[c1], %[c1], %[t0]\n"
      "v_add_f32 %[c1], %[c1], %[t1]\n"
      "v_mfma_f32_32x32x16_bf16 v[112:127], v[244:247], v[52:55], v[112:127]\n"
      "v_lshlrev_b32 %[t0], 16, v53\n"
      "v_and_b32 %[t1], %[msk], v53\n"
      "v_add_f32 %[c2], %[c2], %[t0]\n"
      "v_add_f32 %[c2], %[c2], %[t1]\n"
      "s_waitcnt lgkmcnt(8)\n"
      "v_mfma_f32_32x32x16_bf16 v[80:95], v[248:251], v[56:59], v[80:95]\n"
      "v_lshlrev_b32 %[t0], 16, v58\n"
      "v_and_b32 %[t1], %[msk], v58\n"
      "v_add_f32 %[c0], %[c0], %[t0]\n"
      "v_add_f32 %[c0], %[c0], %[t1]\n"
      "v_mfma_f32_32x32x16_bf16 v[96:111], v[248:251], v[60:63], v[96:111]\n"
      "v_lshlrev_b32 %[t0], 16, v62\n"
      "v_and_b32 %[t1], %[msk], v62\n"
      "v_add_f32 %[c1], %[c1], %[t0]\n"
      "v_add_f32 %[c1], %[c1], %[t1]\n"
      "s_waitcnt lgkmcnt(4)\n"
      "v_mfma_f32_32x32x16_bf16 v[112:127], v[248:251], v[64:67], v[112:127]\n"
      "v_lshlrev_b32 %[t0], 16, v66\n"
      "v_and_b32 %[t1], %[msk], v66\n"
      "v_add_f32 %[c2], %[c2], %[t0]\n"
      "v_add_f32 %[c2], %[c2], %[t1]\n"
      "v_mfma_f32_32x32x16_bf16 v[80:95], v[252:255], v[68:71], v[80:95]\n"
      "v_lshlrev_b32 %[t0], 16, v71\n"
      "v_and_b32 %[t1], %[msk], v71\n"
      "v_add_f32 %[c0], %[c0], %[t0]\n"
      "v_add_f32 %[c0], %[c0], %[t1]\n"
      "s_waitcnt lgkmcnt(0)\n"
      "v_mfma_f32_32x32x16_bf16 v[96:111], v[252:255], v[72:75], v[96:111]\n"
      "v_lshlrev_b32 %[t0], 16, v75\n"
      "v_and_b32 %[t1], %[msk], v75\n"
      "v_add_f32 %[c1], %[c1], %[t0]\n"
      "v_add_f32 %[c1], %[c1], %[t1]\n"
      "v_mfma_f32_32x32x16_bf16 v[112:127], v[252:255], v[76:79], v[112:127]\n"
      "v_lshlrev_b32 %[t0], 16, v79\n"
      "v_and_b32 %[t1], %[msk], v79\n"
      "v_add_f32 %[c2], %[c2], %[t0]\n"
      "v_add_f32 %[c2], %[c2], %[t1]\n"
      "s_nop 15\n"
      "3:\n"
      "s_waitcnt vmcnt(0)\n"
        : "+{v[80:95]}"(a0), "+{v[96:111]}"(a1), "+{v[112:127]}"(a2), CSUM, "+v"(rsw) : XIN_HI, B4, [msk] "s"(msk), [cg] "s"(cg) : QCLOB, "scc", "memory");
    RELANE();
    c0 = half_pick(c0, c1); c2 = half_sum(c2);
    lds_wf1(SCR_CS + lane * 16 + m * 4, c0);
    if (h == 0) lds_wf1(SCR_CS + (64 + t) * 16 + m * 4, c2);
    ROWSTATS();
    lds_wf1(SCR_RA + (32 * m + t) * 4, ra);
    float sc[3];
    unsigned so[3], sd[3];
#pragma unroll
    for (int u = 0; u < 3; u++) {
      const int blk = 3 * cg + u, typ = blk >> 4, hp = blk & 15;
      sc[u] = typ == 0 ? ra * (0.25f * 1.4426950408889634f) : ra;
      sd[u] = typ == 0 ? 65536u : typ == 1 ? 131072u : 512u;
      so[u] = typ == 2 ? WS_KV + (2 * hp + (t >> 4)) * 131072 + tile * 2048 + 1024 + h * 256 + (t & 15) * 16
                       : (typ == 0 ? WS_Q + tile * 1024 : WS_KV + tile * 2048) + 2 * hp * sd[u] + lane * 16;
    }
    unsigned pa = SCR_CS + t * 16, rp = SCR_RA + (32 * m + 4 * h) * 4;
    asm volatile("s_waitcnt lgkmcnt(0)\n\ts_barrier" : "+v"(sc[0]), "+v"(sc[1]), "+v"(sc[2]), "+v"(so[0]), "+v"(so[1]), "+v"(so[2]), "+v"(pa), "+v"(rp), "+{v[44:47]}"(rowf) :: "memory");
    asm volatile(
      "s_cmp_gt_u32 %[cg], 10\n"
      "s_cbranch_scc1 2f\n"
      "s_cmp_eq_u32 %[cg], 10\n"
      "s_cbranch_scc1 1f\n"
      "ds_read_b128 v[48:51], %[pa] offset:0\n"
      "ds_read_b128 v[52:55], %[pa] offset:512\n"
      "ds_read_b128 v[56:59], %[pa] offset:1024\n"
      "v_mov_b32 v33, 0\n"
      "v_mov_b64 v[34:35], 0\n"
      "v_mov_b32 v37, 0\n"
      "v_mov_b64 v[38:39], 0\n"
      "v_mov_b32 v41, 0\n"
      "v_mov_b64 v[42:43], 0\n"
      "s_waitcnt lgkmcnt(0)\n"
      "v_add_f32 v48, v48, v49\n"
      "v_add_f32 v52, v52, v53\n"
      "v_add_f32 v56, v56, v57\n"
      "v_add_f32 v50, v50, v51\n"
      "v_add_f32 v54, v54, v55\n"
      "v_add_f32 v58, v58, v59\n"
      "v_add_f32 v48, v48, v50\n"
      "v_add_f32 v52, v52, v54\n"
      "v_add_f32 v56, v56, v58\n"
      "v_cvt_pk_bf16_f32 v49, v48, v48\n"
      "v_cvt_pk_bf16_f32 v53, v52, v52\n"
      "v_cvt_pk_bf16_f32 v57, v56, v56\n"
      "v_and_b32 v49, %[msk], v49\n"
      "v_and_b32 v53, %[msk], v53\n"
      "v_and_b32 v57, %[msk], v57\n"
      "v_sub_f32 v48, v48, v49\n"
      "v_sub_f32 v52, v52, v53\n"
      "v_sub_f32 v56, v56, v57\n"
      "v_cvt_pk_bf16_f32 v48, v49, v48\n"
      "v_cvt_pk_bf16_f32 v52, v53, v52\n"
      "v_cvt_pk_bf16_f32 v56, v57, v56\n"
      "v_cndmask_b32 v32, 0, v48, %[low]\n"
      "v_cndmask_b32 v36, 0, v52, %[low]\n"
      "v_cndmask_b32 v40, 0, v56, %[low]\n"
      "s_nop 0\n"
      "v_mfma_f32_32x32x16_bf16 v[80:95], v[32:35], v[44:47], v[80:95]\n"
      "v_mfma_f32_32x32x16_bf16 v[96:111], v[36:39], v[44:47], v[96:111]\n"
      "v_mfma_f32_32x32x16_bf16 v[112:127], v[40:43], v[44:47], v[112:127]\n"
      "s_waitcnt lgkmcnt(0)\n"
      "s_barrier\n"
      "s_nop 7\n"
      "v_mul_f32 v80, %[s0], v80\n"
      "v_mul_f32 v81, %[s0], v81\n"
      "v_mul_f32 v82, %[s0], v82\n"
      "v_mul_f32 v83, %[s0], v83\n"
      "v_mul_f32 v84, %[s0], v84\n"
      "v_mul_f32 v85, %[s0], v85\n"
      "v_mul_f32 v86, %[s0], v86\n"
      "v_mul_f32 v87, %[s0], v87\n"
      "v_mul_f32 v88, %[s0], v88\n"
      "v_mul_f32 v89, %[s0], v89\n"
      "v_mul_f32 v90, %[s0], v90\n"
      "v_mul_f32 v91, %[s0], v91\n"
      "v_mul_f32 v92, %[s0], v92\n"
      "v_mul_f32 v93, %[s0], v93\n"
      "v_mul_f32 v94, %[s0], v94\n"
      "v_mul_f32 v95, %[s0], v95\n"
      "v_cvt_pk_bf16_f32 v80, v80, v81\n"
      "v_cvt_pk_bf16_f32 v81, v82, v83\n"
      "v_cvt_pk_bf16_f32 v82, v84, v85\n"
      "v_cvt_pk_bf16_f32 v83, v86, v87\n"
      "v_cvt_pk_bf16_f32 v84, v88, v89\n"
      "v_cvt_pk_bf16_f32 v85, v90, v91\n"
      "v_cvt_pk_bf16_f32 v86, v92, v93\n"
      "v_cvt_pk_bf16_f32 v87, v94, v95\n"
      "v_add_u32 v48, %[d0], %[o0]\n"
      "global_store_dwordx4 %[o0], v[80:83], %[ws] sc1\n"
      "global_store_dwordx4 v48, v[84:87], %[ws] sc1\n"
      "v_mul_f32 v96, %[s1], v96\n"
      "v_mul_f32 v97, %[s1], v97\n"
      "v_mul_f32 v98, %[s1], v98\n"
      "v_mul_f32 v99, %[s1], v99\n"
      "v_mul_f32 v100, %[s1], v100\n"
      "v_mul_f32 v101, %[s1], v101\n"
      "v_mul_f32 v102, %[s1], v102\n"
      "v_mul_f32 v103, %[s1], v103\n"
      "v_mul_f32 v104, %[s1], v104\n"
      "v_mul_f32 v105, %[s1], v105\n"
      "v_mul_f32 v106, %[s1], v106\n"
      "v_mul_f32 v107, %[s1], v107\n"
      "v_mul_f32 v108, %[s1], v108\n"
      "v_mul_f32 v109, %[s1], v109\n"
      "v_mul_f32 v110, %[s1], v110\n"
      "v_mul_f32 v111, %[s1], v111\n"
      "v_cvt_pk_bf16_f32 v96, v96, v97\n"
      "v_cvt_pk_bf16_f32 v97, v98, v99\n"
      "v_cvt_pk_bf16_f32 v98, v100, v101\n"
      "v_cvt_pk_bf16_f32 v99, v102, v103\n"
      "v_cvt_pk_bf16_f32 v100, v104, v105\n"
      "v_cvt_pk_bf16_f32 v101, v106, v107\n"
      "v_cvt_pk_bf16_f32 v102, v108, v109\n"
      "v_cvt_pk_bf16_f32 v103, v110, v111\n"
      "v_add_u32 v49, %[d1], %[o1]\n"
      "global_store_dwordx4 %[o1], v[96:99], %[ws] sc1\n"
      "global_store_dwordx4 v49, v[100:103], %[ws] sc1\n"
      "v_mul_f32 v112, %[s2], v112\n"
      "v_mul_f32 v113, %[s2], v113\n"
      "v_mul_f32 v114, %[s2], v114\n"
      "v_mul_f32 v115, %[s2], v115\n"
      "v_mul_f32 v116, %[s2], v116\n"
      "v_mul_f32 v117, %[s2], v117\n"
      "v_mul_f32 v118, %[s2], v118\n"
      "v_mul_f32 v119, %[s2], v119\n"
      "v_mul_f32 v120, %[s2], v120\n"
      "v_mul_f32 v121, %[s2], v121\n"
      "v_mul_f32 v122, %[s2], v122\n"
      "v_mul_f32 v123, %[s2], v123\n"
      "v_mul_f32 v124, %[s2], v124\n"
      "v_mul_f32 v125, %[s2], v125\n"
      "v_mul_f32 v126, %[s2], v126\n"
      "v_mul_f32 v127, %[s2], v127\n"
      "v_cvt_pk_bf16_f32 v112, v112, v113\n"
      "v_cvt_pk_bf16_f32 v113, v114, v115\n"
      "v_cvt_pk_bf16_f32 v114, v116, v117\n"
      "v_cvt_pk_bf16_f32 v115, v118, v119\n"
      "v_cvt_pk_bf16_f32 v116, v120, v121\n"
      "v_cvt_pk_bf16_f32 v117, v122, v123\n"
      "v_cvt_pk_bf16_f32 v118, v124, v125\n"
      "v_cvt_pk_bf16_f32 v119, v126, v127\n"
      "v_add_u32 v50, %[d2], %[o2]\n"
      "global_store_dwordx4 %[o2], v[112:115], %[ws] sc1\n"
      "global_store_dwordx4 v50, v[116:119], %[ws] sc1\n"
      "s_nop 1\n"
      "s_branch 3f\n"
      "1:\n"
      "ds_read_b128 v[48:51], %[pa] offset:0\n"
      "ds_read_b128 v[52:55], %[pa] offset:512\n"
      "ds_read_b128 v[56:59], %[pa] offset:1024\n"
      "ds_read_b128 v[60:63], %[ra] offset:0\n"
      "ds_read_b128 v[64:67], %[ra] offset:32\n"
      "ds_read_b128 v[68:71], %[ra] offset:64\n"
      "ds_read_b128 v[72:75], %[ra] offset:96\n"
      "v_mov_b32 v33, 0\n"
      "v_mov_b64 v[34:35], 0\n"
      "v_mov_b32 v37, 0\n"
      "v_mov_b64 v[38:39], 0\n"
      "v_mov_b32 v41, 0\n"
      "v_mov_b64 v[42:43], 0\n"
      "s_waitcnt lgkmcnt(4)\n"
      "v_add_f32 v48, v48, v49\n"
      "v_add_f32 v52, v52, v53\n"
      "v_add_f32 v56, v56, v57\n"
      "v_add_f32 v50, v50, v51\n"
      "v_add_f32 v54, v54, v55\n"
      "v_add_f32 v58, v58, v59\n"
      "v_add_f32 v48, v48, v50\n"
      "v_add_f32 v52, v52, v54\n"
      "v_add_f32 v56, v56, v58\n"
      "v_cvt_pk_bf16_f32 v49, v48, v48\n"
      "v_cvt_pk_bf16_f32 v53, v52, v52\n"
      "v_cvt_pk_bf16_f32 v57, v56, v56\n"
      "v_and_b32 v49, %[msk], v49\n"
      "v_and_b32 v53, %[msk], v53\n"
      "v_and_b32 v57, %[msk], v57\n"
      "v_sub_f32 v48, v48, v49\n"
      "v_sub_f32 v52, v52, v53\n"
      "v_sub_f32 v56, v56, v57\n"
      "v_cvt_pk_bf16_f32 v48, v49, v48\n"
      "v_cvt_pk_bf16_f32 v52, v53, v52\n"
      "v_cvt_pk_bf16_f32 v56, v57, v56\n"
      "v_cndmask_b32 v32, 0, v48, %[low]\n"
      "v_cndmask_b32 v36, 0, v52, %[low]\n"
      "v_cndmask_b32 v40, 0, v56, %[low]\n"
      "s_nop 0\n"
      "v_mfma_f32_32x32x16_bf16 v[80:95], v[32:35], v[44:47], v[80:95]\n"
      "v_mfma_f32_32x32x16_bf16 v[96:111], v[36:39], v[44:47], v[96:111]\n"
      "v_mfma_f32_32x32x16_bf16 v[112:127], v[44:47], v[40:43], v[112:127]\n"
      "s_waitcnt lgkmcnt(0)\n"
      "s_barrier\n"
      "s_nop 7\n"
      "v_mul_f32 v80, %[s0], v80\n"
      "v_mul_f32 v81, %[s0], v81\n"
      "v_mul_f32 v82, %[s0], v82\n"
      "v_mul_f32 v83, %[s0], v83\n"
      "v_mul_f32 v84, %[s0], v84\n"
      "v_mul_f32 v85, %[s0], v85\n"
      "v_mul_f32 v86, %[s0], v86\n"
      "v_mul_f32 v87, %[s0], v87\n"
      "v_mul_f32 v88, %[s0], v88\n"
      "v_mul_f32 v89, %[s0], v89\n"
      "v_mul_f32 v90, %[s0], v90\n"
      "v_mul_f32 v91, %[s0], v91\n"
      "v_mul_f32 v92, %[s0], v92\n"
      "v_mul_f32 v93, %[s0], v93\n"
      "v_mul_f32 v94, %[s0], v94\n"
      "v_mul_f32 v95, %[s0], v95\n"
      "v_cvt_pk_bf16_f32 v80, v80, v81\n"
      "v_cvt_pk_bf16_f32 v81, v82, v83\n"
      "v_cvt_pk_bf16_f32 v82, v84, v85\n"
      "v_cvt_pk_bf16_f32 v83, v86, v87\n"
      "v_cvt_pk_bf16_f32 v84, v88, v89\n"
      "v_cvt_pk_bf16_f32 v85, v90, v91\n"
      "v_cvt_pk_bf16_f32 v86, v92, v93\n"
      "v_cvt_pk_bf16_f32 v87, v94, v95\n"
      "v_add_u32 v48, %[d0], %[o0]\n"
      "global_store_dwordx4 %[o0], v[80:83], %[ws] sc1\n"
      "global_store_dwordx4 v48, v[84:87], %[ws] sc1\n"
      "v_mul_f32 v96, %[s1], v96\n"
      "v_mul_f32 v97, %[s1], v97\n"
      "v_mul_f32 v98, %[s1], v98\n"
      "v_mul_f32 v99, %[s1], v99\n"
      "v_mul_f32 v100, %[s1], v100\n"
      "v_mul_f32 v101, %[s1], v101\n"
      "v_mul_f32 v102, %[s1], v102\n"
      "v_mul_f32 v103, %[s1], v103\n"
      "v_mul_f32 v104, %[s1], v104\n"
      "v_mul_f32 v105, %[s1], v105\n"
      "v_mul_f32 v106, %[s1], v106\n"
      "v_mul_f32 v107, %[s1], v107\n"
      "v_mul_f32 v108, %[s1], v108\n"
      "v_mul_f32 v109, %[s1], v109\n"
      "v_mul_f32 v110, %[s1], v110\n"
      "v_mul_f32 v111, %[s1], v111\n"
      "v_cvt_pk_bf16_f32 v96, v96, v97\n"
      "v_cvt_pk_bf16_f32 v97, v98, v99\n"
      "v_cvt_pk_bf16_f32 v98, v100, v101\n"
      "v_cvt_pk_bf16_f32 v99, v102, v103\n"
      "v_cvt_pk_bf16_f32 v100, v104, v105\n"
      "v_cvt_pk_bf16_f32 v101, v106, v107\n"
      "v_cvt_pk_bf16_f32 v102, v108, v109\n"
      "v_cvt_pk_bf16_f32 v103, v110, v111\n"
      "v_add_u32 v49, %[d1], %[o1]\n"
      "global_store_dwordx4 %[o1], v[96:99], %[ws] sc1\n"
      "global_store_dwordx4 v49, v[100:103], %[ws] sc1\n"
      "v_mul_f32 v112, v60, v112\n"
      "v_mul_f32 v113, v61, v113\n"
      "v_mul_f32 v114, v62, v114\n"
      "v_mul_f32 v115, v63, v115\n"
      "v_mul_f32 v116, v64, v116\n"
      "v_mul_f32 v117, v65, v117\n"
      "v_mul_f32 v118, v66, v118\n"
      "v_mul_f32 v119, v67, v119\n"
      "v_mul_f32 v120, v68, v120\n"
      "v_mul_f32 v121, v69, v121\n"
      "v_mul_f32 v122, v70, v122\n"
      "v_mul_f32 v123, v71, v123\n"
      "v_mul_f32 v124, v72, v124\n"
      "v_mul_f32 v125, v73, v125\n"
      "v_mul_f32 v126, v74, v126\n"
      "v_mul_f32 v127, v75, v127\n"
      "v_cvt_pk_bf16_f32 v112, v112, v113\n"
      "v_cvt_pk_bf16_f32 v113, v114, v115\n"
      "v_cvt_pk_bf16_f32 v114, v116, v117\n"
      "v_cvt_pk_bf16_f32 v115, v118, v119\n"
      "v_cvt_pk_bf16_f32 v116, v120, v121\n"
      "v_cvt_pk_bf16_f32 v117, v122, v123\n"
      "v_cvt_pk_bf16_f32 v118, v124, v125\n"
      "v_cvt_pk_bf16_f32 v119, v126, v127\n"
      "v_add_u32 v50, %[d2], %[o2]\n"
      "global_store_dwordx4 %[o2], v[112:115], %[ws] sc1\n"
      "global_store_dwordx4 v50, v[116:119], %[ws] sc1\n"
      "s_nop 1\n"
      "s_branch 3f\n"
      "2:\n"
      "ds_read_b128 v[48:51], %[pa] offset:0\n"
      "ds_read_b128 v[52:55], %[pa] offset:512\n"
      "ds_read_b128 v[56:59], %[pa] offset:1024\n"
      "ds_read_b128 v[60:63], %[ra] offset:0\n"
      "ds_read_b128 v[64:67], %[ra] offset:32\n"
      "ds_read_b128 v[68:71], %[ra] offset:64\n"
      "ds_read_b128 v[72:75], %[ra] offset:96\n"
      "v_mov_b32 v33, 0\n"
      "v_mov_b64 v[34:35], 0\n"
      "v_mov_b32 v37, 0\n"
      "v_mov_b64 v[38:39], 0\n"
      "v_mov_b32 v41, 0\n"
      "v_mov_b64 v[42:43], 0\n"
      "s_waitcnt lgkmcnt(4)\n"
      "v_add_f32 v48, v48, v49\n"
      "v_add_f32 v52, v52, v53\n"
      "v_add_f32 v56, v56, v57\n"
      "v_add_f32 v50, v50, v51\n"
      "v_add_f32 v54, v54, v55\n"
      "v_add_f32 v58, v58, v59\n"
      "v_add_f32 v48, v48, v50\n"
      "v_add_f32 v52, v52, v54\n"
      "v_add_f32 v56, v56, v58\n"
      "v_cvt_pk_bf16_f32 v49, v48, v48\n"
      "v_cvt_pk_bf16_f32 v53, v52, v52\n"
      "v_cvt_pk_bf16_f32 v57, v56, v56\n"
      "v_and_b32 v49, %[msk], v49\n"
      "v_and_b32 v53, %[msk], v53\n"
      "v_and_b32 v57, %[msk], v57\n"
      "v_sub_f32 v48, v48, v49\n"
      "v_sub_f32 v52, v52, v53\n"
      "v_sub_f32 v56, v56, v57\n"
      "v_cvt_pk_bf16_f32 v48, v49, v48\n"
      "v_cvt_pk_bf16_f32 v52, v53, v52\n"
      "v_cvt_pk_bf16_f32 v56, v57, v56\n"
      "v_cndmask_b32 v32, 0, v48, %[low]\n"
      "v_cndmask_b32 v36, 0, v52, %[low]\n"
      "v_cndmask_b32 v40, 0, v56, %[low]\n"
      "s_nop 0\n"
      "v_mfma_f32_32x32x16_bf16 v[80:95], v[44:47], v[32:35], v[80:95]\n"
      "v_mfma_f32_32x32x16_bf16 v[96:111], v[44:47], v[36:39], v[96:111]\n"
      "v_mfma_f32_32x32x16_bf16 v[112:127], v[44:47], v[40:43], v[112:127]\n"
      "s_waitcnt lgkmcnt(0)\n"
      "s_barrier\n"
      "s_nop 7\n"
      "v_mul_f32 v80, v60, v80\n"
      "v_mul_f32 v81, v61, v81\n"
      "v_mul_f32 v82, v62, v82\n"
      "v_mul_f32 v83, v63, v83\n"
      "v_mul_f32 v84, v64, v84\n"
      "v_mul_f32 v85, v65, v85\n"
      "v_mul_f32 v86, v66, v86\n"
      "v_mul_f32 v87, v67, v87\n"
      "v_mul_f32 v88, v68, v88\n"
      "v_mul_f32 v89, v69, v89\n"
      "v_mul_f32 v90, v70, v90\n"
      "v_mul_f32 v91, v71, v91\n"
      "v_mul_f32 v92, v72, v92\n"
      "v_mul_f32 v93, v73, v93\n"
      "v_mul_f32 v94, v74, v94\n"
      "v_mul_f32 v95, v75, v95\n"
      "v_cvt_pk_bf16_f32 v80, v80, v81\n"
      "v_cvt_pk_bf16_f32 v81, v82, v83\n"
      "v_cvt_pk_bf16_f32 v82, v84, v85\n"
      "v_cvt_pk_bf16_f32 v83, v86, v87\n"
      "v_cvt_pk_bf16_f32 v84, v88, v89\n"
      "v_cvt_pk_bf16_f32 v85, v90, v91\n"
      "v_cvt_pk_bf16_f32 v86, v92, v93\n"
      "v_cvt_pk_bf16_f32 v87, v94, v95\n"
      "v_add_u32 v48, %[d0], %[o0]\n"
      "global_store_dwordx4 %[o0], v[80:83], %[ws] sc1\n"
      "global_store_dwordx4 v48, v[84:87], %[ws] sc1\n"
      "v_mul_f32 v96, v60, v96\n"
      "v_mul_f32 v97, v61, v97\n"
      "v_mul_f32 v98, v62, v98\n"
      "v_mul_f32 v99, v63, v99\n"
      "v_mul_f32 v100, v64, v100\n"
      "v_mul_f32 v101, v65, v101\n"
      "v_mul_f32 v102, v66, v102\n"
      "v_mul_f32 v103, v67, v103\n"
      "v_mul_f32 v104, v68, v104\n"
      "v_mul_f32 v105, v69, v105\n"
      "v_mul_f32 v106, v70, v106\n"
      "v_mul_f32 v107, v71, v107\n"
      "v_mul_f32 v108, v72, v108\n"
      "v_mul_f32 v109, v73, v109\n"
      "v_mul_f32 v110, v74, v110\n"
      "v_mul_f32 v111, v75, v111\n"
      "v_cvt_pk_bf16_f32 v96, v96, v97\n"
      "v_cvt_pk_bf16_f32 v97, v98, v99\n"
      "v_cvt_pk_bf16_f32 v98, v100, v101\n"
      "v_cvt_pk_bf16_f32 v99, v102, v103\n"
      "v_cvt_pk_bf16_f32 v100, v104, v105\n"
      "v_cvt_pk_bf16_f32 v101, v106, v107\n"
      "v_cvt_pk_bf16_f32 v102, v108, v109\n"
      "v_cvt_pk_bf16_f32 v103, v110, v111\n"
      "v_add_u32 v49, %[d1], %[o1]\n"
      "global_store_dwordx4 %[o1], v[96:99], %[ws] sc1\n"
      "global_store_dwordx4 v49, v[100:103], %[ws] sc1\n"
      "v_mul_f32 v112, v60, v112\n"
      "v_mul_f32 v113, v61, v113\n"
      "v_mul_f32 v114, v62, v114\n"
      "v_mul_f32 v115, v63, v115\n"
      "v_mul_f32 v116, v64, v116\n"
      "v_mul_f32 v117, v65, v117\n"
      "v_mul_f32 v118, v66, v118\n"
      "v_mul_f32 v119, v67, v119\n"
      "v_mul_f32 v120, v68, v120\n"
      "v_mul_f32 v121, v69, v121\n"
      "v_mul_f32 v122, v70, v122\n"
      "v_mul_f32 v123, v71, v123\n"
      "v_mul_f32 v124, v72, v124\n"
      "v_mul_f32 v125, v73, v125\n"
      "v_mul_f32 v126, v74, v126\n"
      "v_mul_f32 v127, v75, v127\n"
      "v_cvt_pk_bf16_f32 v112, v112, v113\n"
      "v_cvt_pk_bf16_f32 v113, v114, v115\n"
      "v_cvt_pk_bf16_f32 v114, v116, v117\n"
      "v_cvt_pk_bf16_f32 v115, v118, v119\n"
      "v_cvt_pk_bf16_f32 v116, v120, v121\n"
      "v_cvt_pk_bf16_f32 v117, v122, v123\n"
      "v_cvt_pk_bf16_f32 v118, v124, v125\n"
      "v_cvt_pk_bf16_f32 v119, v126, v127\n"
      "v_add_u32 v50, %[d2], %[o2]\n"
      "global_store_dwordx4 %[o2], v[112:115], %[ws] sc1\n"
      "global_store_dwordx4 v50, v[116:119], %[ws] sc1\n"
      "s_nop 1\n"
      "3:\n"
        : "+{v[80:95]}"(a0), "+{v[96:111]}"(a1), "+{v[112:127]}"(a2)
        : "{v[44:47]}"(rowf), [pa] "v"(pa), [ra] "v"(rp), [s0] "v"(sc[0]), [s1] "v"(sc[1]), [s2] "v"(sc[2]), [o0] "v"(so[0]), [o1] "v"(so[1]), [o2] "v"(so[2]),
          [d0] "s"(sd[0]), [d1] "s"(sd[1]), [d2] "s"(sd[2]), [ws] "s"(ws), [msk] "s"(msk), [low] "s"(0xffffffffull), [cg] "s"(cg)
        : "v32", "v33", "v34", "v35", "v36", "v37", "v38", "v39", "v40", "v41", "v42", "v43", "v48", "v49", "v50", "v51", "v52", "v53", "v54", "v55", "v56", "v57", "v58", "v59", "v60", "v61", "v62", "v63", "v64", "v65", "v66", "v67", "v68", "v69", "v70", "v71", "v72", "v73", "v74", "v75", "scc", "memory");
    __builtin_amdgcn_s_setprio(0);
    VMCNT(0);
    arrive(C_ST, ws, fq, tag);
    if (ffn) {
      const unsigned fv = (lane >> 4) * 3584 + (((lane & 15) ^ ((lane >> 4) << 2)) << 4);
#pragma unroll
      for (int i = 0; i < 16; i++) { const int g = 16 * m + ((i + 4 * wr) & 15); dma16(w1b + (size_t)(64 + g) * 14336, fv, L_FW2 + g * 1024); }
      VMCNT(0);
    }
    lds_add(C_FW2);
    {
      const int hp = head >> 1;
      const unsigned off = lane < 16 ? WS_FQ + (((16 + hp) / 3) * 16 + lane) * 8 : lane < 32 ? WS_FQ + (((32 + hp) / 3) * 16 + lane - 16) * 8
                         : lane < 34 ? WS_FQ + ((hp / 3) * 16 + 2 * qb + lane - 32) * 8 : 0xffffffffu;
      if (w == 0) { wait_flags(ws, off, tag); lds_add(C_FQ); } else lds_wait(C_FQ, 1);
    }
    u32x4 qf[2];
    const char* qp = ws + WS_Q + (size_t)(head * 64 + qb * 8 + 2 * m) * 1024;
    gload_sc(qf[0], qp, lane * 16); gload_sc(qf[1], qp + 1024, lane * 16);
    lds_wait(C_F1, 4);
#pragma unroll
    for (int i = 0; i < 16; i++) { const unsigned o = (m + (i & 1) * 4) * 1024; dma16_sc(kvp + KVSRC(i >> 1) + o, lane * 16, L_KV + (i >> 1) * 8192 + o); }
    const unsigned ka = L_KV + lane * 16;
    const unsigned va = t < 16 ? L_KV + 1024 + h * 256 + t * 16 : (t == 16 || t == 20) ? MISC : MISC + 16, vs = t < 16 ? 8192u : 0u;
    AttnState st;
    f32x16 na, nb;
    attn_init(st);
    asm volatile("" : "={v[136:151]}"(na), "={v[152:167]}"(nb));
    asm volatile("s_waitcnt vmcnt(0)" : "+v"(qf[0]), "+v"(qf[1]) :: "memory");
    lds_add(C_KV1);
    lds_wait(C_KV1, 4);
    attn_loop(qf[0], qf[1], na, nb, 0, ka, va, vs, st);
    if (__builtin_expect(!__all(st.oa[8] < 1.0e12f && st.ob[8] < 1.0e12f), 0)) {
      const bf16x8 qa = __builtin_bit_cast(bf16x8, qf[0]), qbf = __builtin_bit_cast(bf16x8, qf[1]);
      float ma = -3.0e38f, mb = -3.0e38f;
#pragma nounroll
      for (int i = 0; i < 64; i++) {
        const bf16x8 kf = lds_b128(ka + i * 2048);
        const f32x16 z = {};
        ma = max16(MFMA(kf, qa, z), ma); mb = max16(MFMA(kf, qbf, z), mb);
      }
      ma = half_max(ma); mb = half_max(mb);
#pragma unroll
      for (int i = 0; i < 16; i++) { na[i] = -ma; nb[i] = -mb; }
      attn_init(st);
      attn_loop(qf[0], qf[1], na, nb, 1, ka, va, vs, st);
    }
    const f32x16 &oa = st.oa, &ob = st.ob;
    const float ia = __builtin_amdgcn_rcpf(oa[8]), ib = __builtin_amdgcn_rcpf(ob[8]);
    u32x4 o0, o1;
#pragma unroll
    for (int j = 0; j < 4; j++) { o0[j] = pk2(oa[2 * j] * ia, oa[2 * j + 1] * ia); o1[j] = pk2(ob[2 * j] * ib, ob[2 * j + 1] * ib); }
    const unsigned base = WS_O + ((qb * 8 + 2 * w) * 32 + head) * 1024 + lane * 16;
    gstore_sc(ws, base, o0); gstore_sc(ws, base + 32768, o1);
    __builtin_amdgcn_s_barrier();
    if (b == 0 && w == 0) {
#pragma nounroll
      for (int i = 0; i < 4; i++) wait_flags(ws, WS_FQ + (i * 64 + lane) * 8, tag);
      if (lane == 0) put_flag(ws, WS_EPOCH, tag);
    }
    return;
  }
  RELANE();
  unsigned xa[8], t0, t1;
#pragma unroll
  for (int i = 0; i < 8; i++) xa[i] = L_X + (32 * m + t) * 512 + ((((i ^ m) << 1) ^ h ^ (t & 15)) << 4);
  f32x16 X[8];
  asm volatile(
      "ds_read_b128 v[128:131], %[a0] offset:0\n"
      "ds_read_b128 v[132:135], %[a1] offset:0\n"
      "ds_read_b128 v[136:139], %[a2] offset:0\n"
      "ds_read_b128 v[140:143], %[a3] offset:0\n"
      "ds_read_b128 v[144:147], %[a4] offset:0\n"
      "ds_read_b128 v[148:151], %[a5] offset:0\n"
      "ds_read_b128 v[152:155], %[a6] offset:0\n"
      "ds_read_b128 v[156:159], %[a7] offset:0\n"
      "ds_read_b128 v[160:163], %[a0] offset:256\n"
      "ds_read_b128 v[164:167], %[a1] offset:256\n"
      "ds_read_b128 v[168:171], %[a2] offset:256\n"
      "ds_read_b128 v[172:175], %[a3] offset:256\n"
      "ds_read_b128 v[176:179], %[a4] offset:256\n"
      "ds_read_b128 v[180:183], %[a5] offset:256\n"
      "ds_read_b128 v[184:187], %[a6] offset:256\n"
      "ds_read_b128 v[188:191], %[a7] offset:256\n"
      "s_waitcnt lgkmcnt(0)\n"
      : "={v[128:143]}"(X[0]), "={v[144:159]}"(X[1]), "={v[160:175]}"(X[2]), "={v[176:191]}"(X[3]) : XADR : "memory");
  BAR();
  const int tile = rb * 4 + m;
  const char* w1b = (const char*)w1 + cf * 256;
  const unsigned fq = WS_FQ + (cg * 16 + rb) * 8, rso = WS_RS + (rb * 128 + 32 * m + t) * 8;
  const char* kvp = ws + WS_KV + (size_t)head * 131072;
  float ra; bf16x8 rowf;
  u32x2 rsw;
  unsigned bias[4];
  {
    const char* xb2 = (const char*)x + (size_t)(rb * 128 + 32 * m) * 1024 + 512;
    const unsigned xv = h * 1024 + ((t ^ h) << 4);
#define XB2(i) do { const unsigned ii = ((i) + 2 * xr) & 15; dma16(xb2, (xv ^ ((ii & 7) * 0x820)) + (ii >> 3) * 16384, L_X + m * 16384 + ii * 1024); } while (0)
#pragma unroll
    for (int i = 0; i < 8; i++) XB2(i);
    VMCNT(8);
    BAR();
#pragma unroll
    for (int i = 8; i < 16; i++) XB2(i);
    unsigned qo[3];
#pragma unroll
    for (int k = 0; k < 3; k++) { const int c = 64 * ((qm[k] - L_QW - w * 3072) >> 10) + lane, r = c / 12; qo[k] = r * 3072 + (c - r * 12) * 16; }
    const char* wq2 = (const char*)wqkv + cg * 192 + (size_t)(256 + 16 * m) * 3072;
#pragma unroll
    for (int j = 0; j < 12; j++) { const unsigned g = ((j / 3) + wr) & 3; dma16(wq2, qo[j % 3] + g * (64 * 3072), qm[j % 3] - 4 * 3072 + 49152 + g * (4 * 3072)); }
    VMCNT(12);
    BAR();
    asm volatile(
      "ds_read_b128 v[192:195], %[a0] offset:0\n"
      "ds_read_b128 v[196:199], %[a1] offset:0\n"
      "ds_read_b128 v[200:203], %[a2] offset:0\n"
      "ds_read_b128 v[204:207], %[a3] offset:0\n"
      "ds_read_b128 v[208:211], %[a4] offset:0\n"
      "ds_read_b128 v[212:215], %[a5] offset:0\n"
      "ds_read_b128 v[216:219], %[a6] offset:0\n"
      "ds_read_b128 v[220:223], %[a7] offset:0\n"
      "ds_read_b128 v[224:227], %[a0] offset:256\n"
      "ds_read_b128 v[228:231], %[a1] offset:256\n"
      "ds_read_b128 v[232:235], %[a2] offset:256\n"
      "ds_read_b128 v[236:239], %[a3] offset:256\n"
      "ds_read_b128 v[240:243], %[a4] offset:256\n"
      "ds_read_b128 v[244:247], %[a5] offset:256\n"
      "ds_read_b128 v[248:251], %[a6] offset:256\n"
      "ds_read_b128 v[252:255], %[a7] offset:256\n"
      "s_waitcnt lgkmcnt(0)\n"
        : "={v[192:207]}"(X[4]), "={v[208:223]}"(X[5]), "={v[224:239]}"(X[6]), "={v[240:255]}"(X[7]) : XADR : "memory");
    VMCNT(0);
    BAR();
    RELANE();
#pragma unroll
    for (int j = 0; j < 4; j++) asm volatile("global_load_ushort %0, %1, %2" : "=v"(bias[j]) : "v"((unsigned)(cf * 128 + (j ^ m) * 32 + t) * 2u), "s"(b1) : "memory");
    asm volatile("global_load_dwordx2 %0, %1, %2 sc1" : "=v"(rsw) : "v"(rso), "s"(ws) : "memory");
    if (ffn) {
      const unsigned fv = (lane >> 4) * 3584 + (((lane & 15) ^ ((lane >> 4) << 2)) << 4);
#pragma unroll
      for (int i = 0; i < 16; i++) { const int g = 16 * m + ((i + 4 * wr) & 15); dma16(w1b + (size_t)g * 14336, fv, L_FW1 + g * 1024); }
    }
    unsigned pf1 = 0;
    if (m == 3) asm volatile("global_load_dword %0, %1, %2" : "=v"(pf1) : "v"((unsigned)((256 + b) * 3584 + (lane < 28 ? lane : 0) * 128)), "s"(w1) : "memory");
    BAR();
    if (tid < 272) { const unsigned v = (tid & 1) ? 0u : one; lds_wu4(MISC + ((tid >> 2) & 3) * 2048 + ((tid >> 1) & 1) * 512 + (tid & 1) * 16, (u32x4){v, v, v, v}); }
    if (tid >= 320 && tid < 323) lds_wu4(CNT + (tid - 320) * 16, (u32x4){0u, 0u, 0u, 0u});
    BAR();
    asm volatile("s_waitcnt vmcnt(0)" : "+v"(rsw), "+v"(bias[0]), "+v"(bias[1]), "+v"(bias[2]), "+v"(bias[3]), "+v"(pf1) :: "memory");
    lds_add(C_FW1);
    ROWSTATS();
  }
  __builtin_amdgcn_s_setprio(1);
  if (ffn) {
    unsigned gb[16];
#pragma unroll
    for (int i = 0; i < 16; i++) gb[i] = L_FW1 + (((i >> 2) ^ m) * 16 + 8 * h + trq) * 256 + ((((i & 3) ^ m ^ trq) << 6) | trc);
    f32x16 g0, g1, g2, g3;
    float c0 = 0.f, c1 = 0.f, c2 = 0.f, c3 = 0.f;
    lds_wait(C_FW1, 4);
    asm volatile(
      "ds_read_b64_tr_b16 v[32:33], %[g0] offset:0\n"
      "ds_read_b64_tr_b16 v[34:35], %[g0] offset:1024\n"
      "ds_read_b64_tr_b16 v[36:37], %[g1] offset:0\n"
      "ds_read_b64_tr_b16 v[38:39], %[g1] offset:1024\n"
      "ds_read_b64_tr_b16 v[40:41], %[g2] offset:0\n"
      "ds_read_b64_tr_b16 v[42:43], %[g2] offset:1024\n"
      "ds_read_b64_tr_b16 v[44:45], %[g3] offset:0\n"
      "ds_read_b64_tr_b16 v[46:47], %[g3] offset:1024\n"
      "ds_read_b64_tr_b16 v[48:49], %[g4] offset:0\n"
      "ds_read_b64_tr_b16 v[50:51], %[g4] offset:1024\n"
      "ds_read_b64_tr_b16 v[52:53], %[g5] offset:0\n"
      "ds_read_b64_tr_b16 v[54:55], %[g5] offset:1024\n"
      "ds_read_b64_tr_b16 v[56:57], %[g6] offset:0\n"
      "ds_read_b64_tr_b16 v[58:59], %[g6] offset:1024\n"
      "s_waitcnt lgkmcnt(10)\n"
      "v_mfma_f32_32x32x16_bf16 v[64:79], v[32:35], v[128:131], 0\n"
      "ds_read_b64_tr_b16 v[60:61], %[g7] offset:0\n"
      "ds_read_b64_tr_b16 v[62:63], %[g7] offset:1024\n"
      "v_lshlrev_b32 %[t0], 16, v32\n"
      "v_and_b32 %[t1], %[msk], v32\n"
      "v_add_f32 %[c0], %[c0], %[t0]\n"
      "v_add_f32 %[c0], %[c0], %[t1]\n"
      "v_mfma_f32_32x32x16_bf16 v[80:95], v[36:39], v[128:131], 0\n"
      "ds_read_b64_tr_b16 v[32:33], %[g8] offset:0\n"
      "ds_read_b64_tr_b16 v[34:35], %[g8] offset:1024\n"
      "v_lshlrev_b32 %[t0], 16, v37\n"
      "v_and_b32 %[t1], %[msk], v37\n"
      "v_add_f32 %[c1], %[c1], %[t0]\n"
      "v_add_f32 %[c1], %[c1], %[t1]\n"
      "s_waitcnt lgkmcnt(10)\n"
      "v_mfma_f32_32x32x16_bf16 v[96:111], v[40:43], v[128:131], 0\n"
      "ds_read_b64_tr_b16 v[36:37], %[g9] offset:0\n"
      "ds_read_b64_tr_b16 v[38:39], %[g9] offset:1024\n"
      "v_lshlrev_b32 %[t0], 16, v42\n"
      "v_and_b32 %[t1], %[msk], v42\n"
      "v_add_f32 %[c2], %[c2], %[t0]\n"
      "v_add_f32 %[c2], %[c2], %[t1]\n"
      "v_mfma_f32_32x32x16_bf16 v[112:127], v[44:47], v[128:131], 0\n"
      "ds_read_b64_tr_b16 v[40:41], %[g10] offset:0\n"
      "ds_read_b64_tr_b16 v[42:43], %[g10] offset:1024\n"
      "v_lshlrev_b32 %[t0], 16, v47\n"
      "v_and_b32 %[t1], %[msk], v47\n"
      "v_add_f32 %[c3], %[c3], %[t0]\n"
      "v_add_f32 %[c3], %[c3], %[t1]\n"
      "s_waitcnt lgkmcnt(10)\n"
      "v_mfma_f32_32x32x16_bf16 v[64:79], v[48:51], v[132:135], v[64:79]\n"
      "ds_read_b64_tr_b16 v[44:45], %[g11] offset:0\n"
      "ds_read_b64_tr_b16 v[46:47], %[g11] offset:1024\n"
      "v_lshlrev_b32 %[t0], 16, v48\n"
      "v_and_b32 %[t1], %[msk], v48\n"
      "v_add_f32 %[c0], %[c0], %[t0]\n"
      "v_add_f32 %[c0], %[c0], %[t1]\n"
      "v_mfma_f32_32x32x16_bf16 v[80:95], v[52:55], v[132:135], v[80:95]\n"
      "ds_read_b64_tr_b16 v[48:49], %[g12] offset:0\n"
      "ds_read_b64_tr_b16 v[50:51], %[g12] offset:1024\n"
      "v_lshlrev_b32 %[t0], 16, v53\n"
      "v_and_b32 %[t1], %[msk], v53\n"
      "v_add_f32 %[c1], %[c1], %[t0]\n"
      "v_add_f32 %[c1], %[c1], %[t1]\n"
      "s_waitcnt lgkmcnt(10)\n"
      "v_mfma_f32_32x32x16_bf16 v[96:111], v[56:59], v[132:135], v[96:111]\n"
      "ds_read_b64_tr_b16 v[52:53], %[g13] offset:0\n"
      "ds_read_b64_tr_b16 v[54:55], %[g13] offset:1024\n"
      "v_lshlrev_b32 %[t0], 16, v58\n"
      "v_and_b32 %[t1], %[msk], v58\n"
      "v_add_f32 %[c2], %[c2], %[t0]\n"
      "v_add_f32 %[c2], %[c2], %[t1]\n"
      "v_mfma_f32_32x32x16_bf16 v[112:127], v[60:63], v[132:135], v[112:127]\n"
      "ds_read_b64_tr_b16 v[56:57], %[g14] offset:0\n"
      "ds_read_b64_tr_b16 v[58:59], %[g14] offset:1024\n"
      "v_lshlrev_b32 %[t0], 16, v63\n"
      "v_and_b32 %[t1], %[msk], v63\n"
      "v_add_f32 %[c3], %[c3], %[t0]\n"
      "v_add_f32 %[c3], %[c3], %[t1]\n"
      "s_waitcnt lgkmcnt(10)\n"
      "v_mfma_f32_32x32x16_bf16 v[64:79], v[32:35], v[136:139], v[64:79]\n"
      "ds_read_b64_tr_b16 v[60:61], %[g15] offset:0\n"
      "ds_read_b64_tr_b16 v[62:63], %[g15] offset:1024\n"
      "v_lshlrev_b32 %[t0], 16, v32\n"
      "v_and_b32 %[t1], %[msk], v32\n"
      "v_add_f32 %[c0], %[c0], %[t0]\n"
      "v_add_f32 %[c0], %[c0], %[t1]\n"
      "v_mfma_f32_32x32x16_bf16 v[80:95], v[36:39], v[136:139], v[80:95]\n"
      "ds_read_b64_tr_b16 v[32:33], %[g0] offset:16384\n"
      "ds_read_b64_tr_b16 v[34:35], %[g0] offset:17408\n"
      "v_lshlrev_b32 %[t0], 16, v37\n"
      "v_and_b32 %[t1], %[msk], v37\n"
      "v_add_f32 %[c1], %[c1], %[t0]\n"
      "v_add_f32 %[c1], %[c1], %[t1]\n"
      "s_waitcnt lgkmcnt(10)\n"
      "v_mfma_f32_32x32x16_bf16 v[96:111], v[40:43], v[136:139], v[96:111]\n"
      "ds_read_b64_tr_b16 v[36:37], %[g1] offset:16384\n"
      "ds_read_b64_tr_b16 v[38:39], %[g1] offset:17408\n"
      "v_lshlrev_b32 %[t0], 16, v42\n"
      "v_and_b32 %[t1], %[msk], v42\n"
      "v_add_f32 %[c2], %[c2], %[t0]\n"
      "v_add_f32 %[c2], %[c2], %[t1]\n"
      "v_mfma_f32_32x32x16_bf16 v[112:127], v[44:47], v[136:139], v[112:127]\n"
      "ds_read_b64_tr_b16 v[40:41], %[g2] offset:16384\n"
      "ds_read_b64_tr_b16 v[42:43], %[g2] offset:17408\n"
      "v_lshlrev_b32 %[t0], 16, v47\n"
      "v_and_b32 %[t1], %[msk], v47\n"
      "v_add_f32 %[c3], %[c3], %[t0]\n"
      "v_add_f32 %[c3], %[c3], %[t1]\n"
      "s_waitcnt lgkmcnt(10)\n"
      "v_mfma_f32_32x32x16_bf16 v[64:79], v[48:51], v[140:143], v[64:79]\n"
      "ds_read_b64_tr_b16 v[44:45], %[g3] offset:16384\n"
      "ds_read_b64_tr_b16 v[46:47], %[g3] offset:17408\n"
      "v_lshlrev_b32 %[t0], 16, v48\n"
      "v_and_b32 %[t1], %[msk], v48\n"
      "v_add_f32 %[c0], %[c0], %[t0]\n"
      "v_add_f32 %[c0], %[c0], %[t1]\n"
      "v_mfma_f32_32x32x16_bf16 v[80:95], v[52:55], v[140:143], v[80:95]\n"
      "ds_read_b64_tr_b16 v[48:49], %[g4] offset:16384\n"
      "ds_read_b64_tr_b16 v[50:51], %[g4] offset:17408\n"
      "v_lshlrev_b32 %[t0], 16, v53\n"
      "v_and_b32 %[t1], %[msk], v53\n"
      "v_add_f32 %[c1], %[c1], %[t0]\n"
      "v_add_f32 %[c1], %[c1], %[t1]\n"
      "s_waitcnt lgkmcnt(10)\n"
      "v_mfma_f32_32x32x16_bf16 v[96:111], v[56:59], v[140:143], v[96:111]\n"
      "ds_read_b64_tr_b16 v[52:53], %[g5] offset:16384\n"
      "ds_read_b64_tr_b16 v[54:55], %[g5] offset:17408\n"
      "v_lshlrev_b32 %[t0], 16, v58\n"
      "v_and_b32 %[t1], %[msk], v58\n"
      "v_add_f32 %[c2], %[c2], %[t0]\n"
      "v_add_f32 %[c2], %[c2], %[t1]\n"
      "v_mfma_f32_32x32x16_bf16 v[112:127], v[60:63], v[140:143], v[112:127]\n"
      "ds_read_b64_tr_b16 v[56:57], %[g6] offset:16384\n"
      "ds_read_b64_tr_b16 v[58:59], %[g6] offset:17408\n"
      "v_lshlrev_b32 %[t0], 16, v63\n"
      "v_and_b32 %[t1], %[msk], v63\n"
      "v_add_f32 %[c3], %[c3], %[t0]\n"
      "v_add_f32 %[c3], %[c3], %[t1]\n"
      "s_waitcnt lgkmcnt(10)\n"
      "v_mfma_f32_32x32x16_bf16 v[64:79], v[32:35], v[144:147], v[64:79]\n"
      "ds_read_b64_tr_b16 v[60:61], %[g7] offset:16384\n"
      "ds_read_b64_tr_b16 v[62:63], %[g7] offset:17408\n"
      "v_lshlrev_b32 %[t0], 16, v32\n"
      "v_and_b32 %[t1], %[msk], v32\n"
      "v_add_f32 %[c0], %[c0], %[t0]\n"
      "v_add_f32 %[c0], %[c0], %[t1]\n"
      "v_mfma_f32_32x32x16_bf16 v[80:95], v[36:39], v[144:147], v[80:95]\n"
      "ds_read_b64_tr_b16 v[32:33], %[g8] offset:16384\n"
      "ds_read_b64_tr_b16 v[34:35], %[g8] offset:17408\n"
      "v_lshlrev_b32 %[t0], 16, v37\n"
      "v_and_b32 %[t1], %[msk], v37\n"
      "v_add_f32 %[c1], %[c1], %[t0]\n"
      "v_add_f32 %[c1], %[c1], %[t1]\n"
      "s_waitcnt lgkmcnt(10)\n"
      "v_mfma_f32_32x32x16_bf16 v[96:111], v[40:43], v[144:147], v[96:111]\n"
      "ds_read_b64_tr_b16 v[36:37], %[g9] offset:16384\n"
      "ds_read_b64_tr_b16 v[38:39], %[g9] offset:17408\n"
      "v_lshlrev_b32 %[t0], 16, v42\n"
      "v_and_b32 %[t1], %[msk], v42\n"
      "v_add_f32 %[c2], %[c2], %[t0]\n"
      "v_add_f32 %[c2], %[c2], %[t1]\n"
      "v_mfma_f32_32x32x16_bf16 v[112:127], v[44:47], v[144:147], v[112:127]\n"
      "ds_read_b64_tr_b16 v[40:41], %[g10] offset:16384\n"
      "ds_read_b64_tr_b16 v[42:43], %[g10] offset:17408\n"
      "v_lshlrev_b32 %[t0], 16, v47\n"
      "v_and_b32 %[t1], %[msk], v47\n"
      "v_add_f32 %[c3], %[c3], %[t0]\n"
      "v_add_f32 %[c3], %[c3], %[t1]\n"
      "s_waitcnt lgkmcnt(10)\n"
      "v_mfma_f32_32x32x16_bf16 v[64:79], v[48:51], v[148:151], v[64:79]\n"
      "ds_read_b64_tr_b16 v[44:45], %[g11] offset:16384\n"
      "ds_read_b64_tr_b16 v[46:47], %[g11] offset:17408\n"
      "v_lshlrev_b32 %[t0], 16, v48\n"
      "v_and_b32 %[t1], %[msk], v48\n"
      "v_add_f32 %[c0], %[c0], %[t0]\n"
      "v_add_f32 %[c0], %[c0], %[t1]\n"
      "v_mfma_f32_32x32x16_bf16 v[80:95], v[52:55], v[148:151], v[80:95]\n"
      "ds_read_b64_tr_b16 v[48:49], %[g12] offset:16384\n"
      "ds_read_b64_tr_b16 v[50:51], %[g12] offset:17408\n"
      "v_lshlrev_b32 %[t0], 16, v53\n"
      "v_and_b32 %[t1], %[msk], v53\n"
      "v_add_f32 %[c1], %[c1], %[t0]\n"
      "v_add_f32 %[c1], %[c1], %[t1]\n"
      "s_waitcnt lgkmcnt(10)\n"
      "v_mfma_f32_32x32x16_bf16 v[96:111], v[56:59], v[148:151], v[96:111]\n"
      "ds_read_b64_tr_b16 v[52:53], %[g13] offset:16384\n"
      "ds_read_b64_tr_b16 v[54:55], %[g13] offset:17408\n"
      "v_lshlrev_b32 %[t0], 16, v58\n"
      "v_and_b32 %[t1], %[msk], v58\n"
      "v_add_f32 %[c2], %[c2], %[t0]\n"
      "v_add_f32 %[c2], %[c2], %[t1]\n"
      "v_mfma_f32_32x32x16_bf16 v[112:127], v[60:63], v[148:151], v[112:127]\n"
      "ds_read_b64_tr_b16 v[56:57], %[g14] offset:16384\n"
      "ds_read_b64_tr_b16 v[58:59], %[g14] offset:17408\n"
      "v_lshlrev_b32 %[t0], 16, v63\n"
      "v_and_b32 %[t1], %[msk], v63\n"
      "v_add_f32 %[c3], %[c3], %[t0]\n"
      "v_add_f32 %[c3], %[c3], %[t1]\n"
      "s_waitcnt lgkmcnt(10)\n"
      "v_mfma_f32_32x32x16_bf16 v[64:79], v[32:35], v[152:155], v[64:79]\n"
      "ds_read_b64_tr_b16 v[60:61], %[g15] offset:16384\n"
      "ds_read_b64_tr_b16 v[62:63], %[g15] offset:17408\n"
      "v_lshlrev_b32 %[t0], 16, v32\n"
      "v_and_b32 %[t1], %[msk], v32\n"
      "v_add_f32 %[c0], %[c0], %[t0]\n"
      "v_add_f32 %[c0], %[c0], %[t1]\n"
      "v_mfma_f32_32x32x16_bf16 v[80:95], v[36:39], v[152:155], v[80:95]\n"
      "ds_read_b64_tr_b16 v[32:33], %[g0] offset:32768\n"
      "ds_read_b64_tr_b16 v[34:35], %[g0] offset:33792\n"
      "v_lshlrev_b32 %[t0], 16, v37\n"
      "v_and_b32 %[t1], %[msk], v37\n"
      "v_add_f32 %[c1], %[c1], %[t0]\n"
      "v_add_f32 %[c1], %[c1], %[t1]\n"
      "s_waitcnt lgkmcnt(10)\n"
      "v_mfma_f32_32x32x16_bf16 v[96:111], v[40:43], v[152:155], v[96:111]\n"
      "ds_read_b64_tr_b16 v[36:37], %[g1] offset:32768\n"
      "ds_read_b64_tr_b16 v[38:39], %[g1] offset:33792\n"
      "v_lshlrev_b32 %[t0], 16, v42\n"
      "v_and_b32 %[t1], %[msk], v42\n"
      "v_add_f32 %[c2], %[c2], %[t0]\n"
      "v_add_f32 %[c2], %[c2], %[t1]\n"
      "v_mfma_f32_32x32x16_bf16 v[112:127], v[44:47], v[152:155], v[112:127]\n"
      "ds_read_b64_tr_b16 v[40:41], %[g2] offset:32768\n"
      "ds_read_b64_tr_b16 v[42:43], %[g2] offset:33792\n"
      "v_lshlrev_b32 %[t0], 16, v47\n"
      "v_and_b32 %[t1], %[msk], v47\n"
      "v_add_f32 %[c3], %[c3], %[t0]\n"
      "v_add_f32 %[c3], %[c3], %[t1]\n"
      "s_waitcnt lgkmcnt(10)\n"
      "v_mfma_f32_32x32x16_bf16 v[64:79], v[48:51], v[156:159], v[64:79]\n"
      "ds_read_b64_tr_b16 v[44:45], %[g3] offset:32768\n"
      "ds_read_b64_tr_b16 v[46:47], %[g3] offset:33792\n"
      "v_lshlrev_b32 %[t0], 16, v48\n"
      "v_and_b32 %[t1], %[msk], v48\n"
      "v_add_f32 %[c0], %[c0], %[t0]\n"
      "v_add_f32 %[c0], %[c0], %[t1]\n"
      "v_mfma_f32_32x32x16_bf16 v[80:95], v[52:55], v[156:159], v[80:95]\n"
      "ds_read_b64_tr_b16 v[48:49], %[g4] offset:32768\n"
      "ds_read_b64_tr_b16 v[50:51], %[g4] offset:33792\n"
      "v_lshlrev_b32 %[t0], 16, v53\n"
      "v_and_b32 %[t1], %[msk], v53\n"
      "v_add_f32 %[c1], %[c1], %[t0]\n"
      "v_add_f32 %[c1], %[c1], %[t1]\n"
      "s_waitcnt lgkmcnt(10)\n"
      "v_mfma_f32_32x32x16_bf16 v[96:111], v[56:59], v[156:159], v[96:111]\n"
      "ds_read_b64_tr_b16 v[52:53], %[g5] offset:32768\n"
      "ds_read_b64_tr_b16 v[54:55], %[g5] offset:33792\n"
      "v_lshlrev_b32 %[t0], 16, v58\n"
      "v_and_b32 %[t1], %[msk], v58\n"
      "v_add_f32 %[c2], %[c2], %[t0]\n"
      "v_add_f32 %[c2], %[c2], %[t1]\n"
      "v_mfma_f32_32x32x16_bf16 v[112:127], v[60:63], v[156:159], v[112:127]\n"
      "ds_read_b64_tr_b16 v[56:57], %[g6] offset:32768\n"
      "ds_read_b64_tr_b16 v[58:59], %[g6] offset:33792\n"
      "v_lshlrev_b32 %[t0], 16, v63\n"
      "v_and_b32 %[t1], %[msk], v63\n"
      "v_add_f32 %[c3], %[c3], %[t0]\n"
      "v_add_f32 %[c3], %[c3], %[t1]\n"
      "s_waitcnt lgkmcnt(10)\n"
      "v_mfma_f32_32x32x16_bf16 v[64:79], v[32:35], v[160:163], v[64:79]\n"
      "ds_read_b64_tr_b16 v[60:61], %[g7] offset:32768\n"
      "ds_read_b64_tr_b16 v[62:63], %[g7] offset:33792\n"
      "v_lshlrev_b32 %[t0], 16, v32\n"
      "v_and_b32 %[t1], %[msk], v32\n"
      "v_add_f32 %[c0], %[c0], %[t0]\n"
      "v_add_f32 %[c0], %[c0], %[t1]\n"
      "v_mfma_f32_32x32x16_bf16 v[80:95], v[36:39], v[160:163], v[80:95]\n"
      "ds_read_b64_tr_b16 v[32:33], %[g8] offset:32768\n"
      "ds_read_b64_tr_b16 v[34:35], %[g8] offset:33792\n"
      "v_lshlrev_b32 %[t0], 16, v37\n"
      "v_and_b32 %[t1], %[msk], v37\n"
      "v_add_f32 %[c1], %[c1], %[t0]\n"
      "v_add_f32 %[c1], %[c1], %[t1]\n"
      "s_waitcnt lgkmcnt(10)\n"
      "v_mfma_f32_32x32x16_bf16 v[96:111], v[40:43], v[160:163], v[96:111]\n"
      "ds_read_b64_tr_b16 v[36:37], %[g9] offset:32768\n"
      "ds_read_b64_tr_b16 v[38:39], %[g9] offset:33792\n"
      "v_lshlrev_b32 %[t0], 16, v42\n"
      "v_and_b32 %[t1], %[msk], v42\n"
      "v_add_f32 %[c2], %[c2], %[t0]\n"
      "v_add_f32 %[c2], %[c2], %[t1]\n"
      "v_mfma_f32_32x32x16_bf16 v[112:127], v[44:47], v[160:163], v[112:127]\n"
      "ds_read_b64_tr_b16 v[40:41], %[g10] offset:32768\n"
      "ds_read_b64_tr_b16 v[42:43], %[g10] offset:33792\n"
      "v_lshlrev_b32 %[t0], 16, v47\n"
      "v_and_b32 %[t1], %[msk], v47\n"
      "v_add_f32 %[c3], %[c3], %[t0]\n"
      "v_add_f32 %[c3], %[c3], %[t1]\n"
      "s_waitcnt lgkmcnt(10)\n"
      "v_mfma_f32_32x32x16_bf16 v[64:79], v[48:51], v[164:167], v[64:79]\n"
      "ds_read_b64_tr_b16 v[44:45], %[g11] offset:32768\n"
      "ds_read_b64_tr_b16 v[46:47], %[g11] offset:33792\n"
      "v_lshlrev_b32 %[t0], 16, v48\n"
      "v_and_b32 %[t1], %[msk], v48\n"
      "v_add_f32 %[c0], %[c0], %[t0]\n"
      "v_add_f32 %[c0], %[c0], %[t1]\n"
      "v_mfma_f32_32x32x16_bf16 v[80:95], v[52:55], v[164:167], v[80:95]\n"
      "ds_read_b64_tr_b16 v[48:49], %[g12] offset:32768\n"
      "ds_read_b64_tr_b16 v[50:51], %[g12] offset:33792\n"
      "v_lshlrev_b32 %[t0], 16, v53\n"
      "v_and_b32 %[t1], %[msk], v53\n"
      "v_add_f32 %[c1], %[c1], %[t0]\n"
      "v_add_f32 %[c1], %[c1], %[t1]\n"
      "s_waitcnt lgkmcnt(10)\n"
      "v_mfma_f32_32x32x16_bf16 v[96:111], v[56:59], v[164:167], v[96:111]\n"
      "ds_read_b64_tr_b16 v[52:53], %[g13] offset:32768\n"
      "ds_read_b64_tr_b16 v[54:55], %[g13] offset:33792\n"
      "v_lshlrev_b32 %[t0], 16, v58\n"
      "v_and_b32 %[t1], %[msk], v58\n"
      "v_add_f32 %[c2], %[c2], %[t0]\n"
      "v_add_f32 %[c2], %[c2], %[t1]\n"
      "v_mfma_f32_32x32x16_bf16 v[112:127], v[60:63], v[164:167], v[112:127]\n"
      "ds_read_b64_tr_b16 v[56:57], %[g14] offset:32768\n"
      "ds_read_b64_tr_b16 v[58:59], %[g14] offset:33792\n"
      "v_lshlrev_b32 %[t0], 16, v63\n"
      "v_and_b32 %[t1], %[msk], v63\n"
      "v_add_f32 %[c3], %[c3], %[t0]\n"
      "v_add_f32 %[c3], %[c3], %[t1]\n"
      "s_waitcnt lgkmcnt(10)\n"
      "v_mfma_f32_32x32x16_bf16 v[64:79], v[32:35], v[168:171], v[64:79]\n"
      "ds_read_b64_tr_b16 v[60:61], %[g15] offset:32768\n"
      "ds_read_b64_tr_b16 v[62:63], %[g15] offset:33792\n"
      "v_lshlrev_b32 %[t0], 16, v32\n"
      "v_and_b32 %[t1], %[msk], v32\n"
      "v_add_f32 %[c0], %[c0], %[t0]\n"
      "v_add_f32 %[c0], %[c0], %[t1]\n"
      "v_mfma_f32_32x32x16_bf16 v[80:95], v[36:39], v[168:171], v[80:95]\n"
      "ds_read_b64_tr_b16 v[32:33], %[g0] offset:49152\n"
      "ds_read_b64_tr_b16 v[34:35], %[g0] offset:50176\n"
      "v_lshlrev_b32 %[t0], 16, v37\n"
      "v_and_b32 %[t1], %[msk], v37\n"
      "v_add_f32 %[c1], %[c1], %[t0]\n"
      "v_add_f32 %[c1], %[c1], %[t1]\n"
      "s_waitcnt lgkmcnt(10)\n"
      "v_mfma_f32_32x32x16_bf16 v[96:111], v[40:43], v[168:171], v[96:111]\n"
      "ds_read_b64_tr_b16 v[36:37], %[g1] offset:49152\n"
      "ds_read_b64_tr_b16 v[38:39], %[g1] offset:50176\n"
      "v_lshlrev_b32 %[t0], 16, v42\n"
      "v_and_b32 %[t1], %[msk], v42\n"
      "v_add_f32 %[c2], %[c2], %[t0]\n"
      "v_add_f32 %[c2], %[c2], %[t1]\n"
      "v_mfma_f32_32x32x16_bf16 v[112:127], v[44:47], v[168:171], v[112:127]\n"
      "ds_read_b64_tr_b16 v[40:41], %[g2] offset:49152\n"
      "ds_read_b64_tr_b16 v[42:43], %[g2] offset:50176\n"
      "v_lshlrev_b32 %[t0], 16, v47\n"
      "v_and_b32 %[t1], %[msk], v47\n"
      "v_add_f32 %[c3], %[c3], %[t0]\n"
      "v_add_f32 %[c3], %[c3], %[t1]\n"
      "s_waitcnt lgkmcnt(10)\n"
      "v_mfma_f32_32x32x16_bf16 v[64:79], v[48:51], v[172:175], v[64:79]\n"
      "ds_read_b64_tr_b16 v[44:45], %[g3] offset:49152\n"
      "ds_read_b64_tr_b16 v[46:47], %[g3] offset:50176\n"
      "v_lshlrev_b32 %[t0], 16, v48\n"
      "v_and_b32 %[t1], %[msk], v48\n"
      "v_add_f32 %[c0], %[c0], %[t0]\n"
      "v_add_f32 %[c0], %[c0], %[t1]\n"
      "v_mfma_f32_32x32x16_bf16 v[80:95], v[52:55], v[172:175], v[80:95]\n"
      "ds_read_b64_tr_b16 v[48:49], %[g4] offset:49152\n"
      "ds_read_b64_tr_b16 v[50:51], %[g4] offset:50176\n"
      "v_lshlrev_b32 %[t0], 16, v53\n"
      "v_and_b32 %[t1], %[msk], v53\n"
      "v_add_f32 %[c1], %[c1], %[t0]\n"
      "v_add_f32 %[c1], %[c1], %[t1]\n"
      "s_waitcnt lgkmcnt(10)\n"
      "v_mfma_f32_32x32x16_bf16 v[96:111], v[56:59], v[172:175], v[96:111]\n"
      "ds_read_b64_tr_b16 v[52:53], %[g5] offset:49152\n"
      "ds_read_b64_tr_b16 v[54:55], %[g5] offset:50176\n"
      "v_lshlrev_b32 %[t0], 16, v58\n"
      "v_and_b32 %[t1], %[msk], v58\n"
      "v_add_f32 %[c2], %[c2], %[t0]\n"
      "v_add_f32 %[c2], %[c2], %[t1]\n"
      "v_mfma_f32_32x32x16_bf16 v[112:127], v[60:63], v[172:175], v[112:127]\n"
      "ds_read_b64_tr_b16 v[56:57], %[g6] offset:49152\n"
      "ds_read_b64_tr_b16 v[58:59], %[g6] offset:50176\n"
      "v_lshlrev_b32 %[t0], 16, v63\n"
      "v_and_b32 %[t1], %[msk], v63\n"
      "v_add_f32 %[c3], %[c3], %[t0]\n"
      "v_add_f32 %[c3], %[c3], %[t1]\n"
      "s_waitcnt lgkmcnt(10)\n"
      "v_mfma_f32_32x32x16_bf16 v[64:79], v[32:35], v[176:179], v[64:79]\n"
      "ds_read_b64_tr_b16 v[60:61], %[g7] offset:49152\n"
      "ds_read_b64_tr_b16 v[62:63], %[g7] offset:50176\n"
      "v_lshlrev_b32 %[t0], 16, v32\n"
      "v_and_b32 %[t1], %[msk], v32\n"
      "v_add_f32 %[c0], %[c0], %[t0]\n"
      "v_add_f32 %[c0], %[c0], %[t1]\n"
      "v_mfma_f32_32x32x16_bf16 v[80:95], v[36:39], v[176:179], v[80:95]\n"
      "ds_read_b64_tr_b16 v[32:33], %[g8] offset:49152\n"
      "ds_read_b64_tr_b16 v[34:35], %[g8] offset:50176\n"
      "v_lshlrev_b32 %[t0], 16, v37\n"
      "v_and_b32 %[t1], %[msk], v37\n"
      "v_add_f32 %[c1], %[c1], %[t0]\n"
      "v_add_f32 %[c1], %[c1], %[t1]\n"
      "s_waitcnt lgkmcnt(10)\n"
      "v_mfma_f32_32x32x16_bf16 v[96:111], v[40:43], v[176:179], v[96:111]\n"
      "ds_read_b64_tr_b16 v[36:37], %[g9] offset:49152\n"
      "ds_read_b64_tr_b16 v[38:39], %[g9] offset:50176\n"
      "v_lshlrev_b32 %[t0], 16, v42\n"
      "v_and_b32 %[t1], %[msk], v42\n"
      "v_add_f32 %[c2], %[c2], %[t0]\n"
      "v_add_f32 %[c2], %[c2], %[t1]\n"
      "v_mfma_f32_32x32x16_bf16 v[112:127], v[44:47], v[176:179], v[112:127]\n"
      "ds_read_b64_tr_b16 v[40:41], %[g10] offset:49152\n"
      "ds_read_b64_tr_b16 v[42:43], %[g10] offset:50176\n"
      "v_lshlrev_b32 %[t0], 16, v47\n"
      "v_and_b32 %[t1], %[msk], v47\n"
      "v_add_f32 %[c3], %[c3], %[t0]\n"
      "v_add_f32 %[c3], %[c3], %[t1]\n"
      "s_waitcnt lgkmcnt(10)\n"
      "v_mfma_f32_32x32x16_bf16 v[64:79], v[48:51], v[180:183], v[64:79]\n"
      "ds_read_b64_tr_b16 v[44:45], %[g11] offset:49152\n"
      "ds_read_b64_tr_b16 v[46:47], %[g11] offset:50176\n"
      "v_lshlrev_b32 %[t0], 16, v48\n"
      "v_and_b32 %[t1], %[msk], v48\n"
      "v_add_f32 %[c0], %[c0], %[t0]\n"
      "v_add_f32 %[c0], %[c0], %[t1]\n"
      "v_mfma_f32_32x32x16_bf16 v[80:95], v[52:55], v[180:183], v[80:95]\n"
      "ds_read_b64_tr_b16 v[48:49], %[g12] offset:49152\n"
      "ds_read_b64_tr_b16 v[50:51], %[g12] offset:50176\n"
      "v_lshlrev_b32 %[t0], 16, v53\n"
      "v_and_b32 %[t1], %[msk], v53\n"
      "v_add_f32 %[c1], %[c1], %[t0]\n"
      "v_add_f32 %[c1], %[c1], %[t1]\n"
      "s_waitcnt lgkmcnt(10)\n"
      "v_mfma_f32_32x32x16_bf16 v[96:111], v[56:59], v[180:183], v[96:111]\n"
      "ds_read_b64_tr_b16 v[52:53], %[g13] offset:49152\n"
      "ds_read_b64_tr_b16 v[54:55], %[g13] offset:50176\n"
      "v_lshlrev_b32 %[t0], 16, v58\n"
      "v_and_b32 %[t1], %[msk], v58\n"
      "v_add_f32 %[c2], %[c2], %[t0]\n"
      "v_add_f32 %[c2], %[c2], %[t1]\n"
      "v_mfma_f32_32x32x16_bf16 v[112:127], v[60:63], v[180:183], v[112:127]\n"
      "ds_read_b64_tr_b16 v[56:57], %[g14] offset:49152\n"
      "ds_read_b64_tr_b16 v[58:59], %[g14] offset:50176\n"
      "v_lshlrev_b32 %[t0], 16, v63\n"
      "v_and_b32 %[t1], %[msk], v63\n"
      "v_add_f32 %[c3], %[c3], %[t0]\n"
      "v_add_f32 %[c3], %[c3], %[t1]\n"
      "s_waitcnt lgkmcnt(10)\n"
      "v_mfma_f32_32x32x16_bf16 v[64:79], v[32:35], v[184:187], v[64:79]\n"
      "ds_read_b64_tr_b16 v[60:61], %[g15] offset:49152\n"
      "ds_read_b64_tr_b16 v[62:63], %[g15] offset:50176\n"
      "v_lshlrev_b32 %[t0], 16, v32\n"
      "v_and_b32 %[t1], %[msk], v32\n"
      "v_add_f32 %[c0], %[c0], %[t0]\n"
      "v_add_f32 %[c0], %[c0], %[t1]\n"
      "v_mfma_f32_32x32x16_bf16 v[80:95], v[36:39], v[184:187], v[80:95]\n"
      "v_lshlrev_b32 %[t0], 16, v37\n"
      "v_and_b32 %[t1], %[msk], v37\n"
      "v_add_f32 %[c1], %[c1], %[t0]\n"
      "v_add_f32 %[c1], %[c1], %[t1]\n"
      "s_waitcnt lgkmcnt(8)\n"
      "v_mfma_f32_32x32x16_bf16 v[96:111], v[40:43], v[184:187], v[96:111]\n"
      "v_lshlrev_b32 %[t0], 16, v42\n"
      "v_and_b32 %[t1], %[msk], v42\n"
      "v_add_f32 %[c2], %[c2], %[t0]\n"
      "v_add_f32 %[c2], %[c2], %[t1]\n"
      "v_mfma_f32_32x32x16_bf16 v[112:127], v[44:47], v[184:187], v[112:127]\n"
      "v_lshlrev_b32 %[t0], 16, v47\n"
      "v_and_b32 %[t1], %[msk], v47\n"
      "v_add_f32 %[c3], %[c3], %[t0]\n"
      "v_add_f32 %[c3], %[c3], %[t1]\n"
      "s_waitcnt lgkmcnt(4)\n"
      "v_mfma_f32_32x32x16_bf16 v[64:79], v[48:51], v[188:191], v[64:79]\n"
      "v_lshlrev_b32 %[t0], 16, v48\n"
      "v_and_b32 %[t1], %[msk], v48\n"
      "v_add_f32 %[c0], %[c0], %[t0]\n"
      "v_add_f32 %[c0], %[c0], %[t1]\n"
      "v_mfma_f32_32x32x16_bf16 v[80:95], v[52:55], v[188:191], v[80:95]\n"
      "v_lshlrev_b32 %[t0], 16, v53\n"
      "v_and_b32 %[t1], %[msk], v53\n"
      "v_add_f32 %[c1], %[c1], %[t0]\n"
      "v_add_f32 %[c1], %[c1], %[t1]\n"
      "s_waitcnt lgkmcnt(0)\n"
      "v_mfma_f32_32x32x16_bf16 v[96:111], v[56:59], v[188:191], v[96:111]\n"
      "v_lshlrev_b32 %[t0], 16, v58\n"
      "v_and_b32 %[t1], %[msk], v58\n"
      "v_add_f32 %[c2], %[c2], %[t0]\n"
      "v_add_f32 %[c2], %[c2], %[t1]\n"
      "v_mfma_f32_32x32x16_bf16 v[112:127], v[60:63], v[188:191], v[112:127]\n"
      "v_lshlrev_b32 %[t0], 16, v63\n"
      "v_and_b32 %[t1], %[msk], v63\n"
      "v_add_f32 %[c3], %[c3], %[t0]\n"
      "v_add_f32 %[c3], %[c3], %[t1]\n"
      "s_nop 15\n"
        : "={v[64:79]}"(g0), "={v[80:95]}"(g1), "={v[96:111]}"(g2), "={v[112:127]}"(g3), CSUM, [c3] "+v"(c3) : XIN_LO, G16, [msk] "s"(msk) : GCLOB, "memory");
    lds_add(C_F1);
#pragma unroll
    for (int i = 0; i < 16; i++) gb[i] += L_FW2 - L_FW1;
    lds_wait(C_FW2, 4);
    asm volatile(
      "ds_read_b64_tr_b16 v[32:33], %[g0] offset:0\n"
      "ds_read_b64_tr_b16 v[34:35], %[g0] offset:1024\n"
      "ds_read_b64_tr_b16 v[36:37], %[g1] offset:0\n"
      "ds_read_b64_tr_b16 v[38:39], %[g1] offset:1024\n"
      "ds_read_b64_tr_b16 v[40:41], %[g2] offset:0\n"
      "ds_read_b64_tr_b16 v[42:43], %[g2] offset:1024\n"
      "ds_read_b64_tr_b16 v[44:45], %[g3] offset:0\n"
      "ds_read_b64_tr_b16 v[46:47], %[g3] offset:1024\n"
      "ds_read_b64_tr_b16 v[48:49], %[g4] offset:0\n"
      "ds_read_b64_tr_b16 v[50:51], %[g4] offset:1024\n"
      "ds_read_b64_tr_b16 v[52:53], %[g5] offset:0\n"
      "ds_read_b64_tr_b16 v[54:55], %[g5] offset:1024\n"
      "ds_read_b64_tr_b16 v[56:57], %[g6] offset:0\n"
      "ds_read_b64_tr_b16 v[58:59], %[g6] offset:1024\n"
      "s_waitcnt lgkmcnt(10)\n"
      "v_mfma_f32_32x32x16_bf16 v[64:79], v[32:35], v[192:195], v[64:79]\n"
      "ds_read_b64_tr_b16 v[60:61], %[g7] offset:0\n"
      "ds_read_b64_tr_b16 v[62:63], %[g7] offset:1024\n"
      "v_lshlrev_b32 %[t0], 16, v32\n"
      "v_and_b32 %[t1], %[msk], v32\n"
      "v_add_f32 %[c0], %[c0], %[t0]\n"
      "v_add_f32 %[c0], %[c0], %[t1]\n"
      "v_mfma_f32_32x32x16_bf16 v[80:95], v[36:39], v[192:195], v[80:95]\n"
      "ds_read_b64_tr_b16 v[32:33], %[g8] offset:0\n"
      "ds_read_b64_tr_b16 v[34:35], %[g8] offset:1024\n"
      "v_lshlrev_b32 %[t0], 16, v37\n"
      "v_and_b32 %[t1], %[msk], v37\n"
      "v_add_f32 %[c1], %[c1], %[t0]\n"
      "v_add_f32 %[c1], %[c1], %[t1]\n"
      "s_waitcnt lgkmcnt(10)\n"
      "v_mfma_f32_32x32x16_bf16 v[96:111], v[40:43], v[192:195], v[96:111]\n"
      "ds_read_b64_tr_b16 v[36:37], %[g9] offset:0\n"
      "ds_read_b64_tr_b16 v[38:39], %[g9] offset:1024\n"
      "v_lshlrev_b32 %[t0], 16, v42\n"
      "v_and_b32 %[t1], %[msk], v42\n"
      "v_add_f32 %[c2], %[c2], %[t0]\n"
      "v_add_f32 %[c2], %[c2], %[t1]\n"
      "v_mfma_f32_32x32x16_bf16 v[112:127], v[44:47], v[192:195], v[112:127]\n"
      "ds_read_b64_tr_b16 v[40:41], %[g10] offset:0\n"
      "ds_read_b64_tr_b16 v[42:43], %[g10] offset:1024\n"
      "v_lshlrev_b32 %[t0], 16, v47\n"
      "v_and_b32 %[t1], %[msk], v47\n"
      "v_add_f32 %[c3], %[c3], %[t0]\n"
      "v_add_f32 %[c3], %[c3], %[t1]\n"
      "s_waitcnt lgkmcnt(10)\n"
      "v_mfma_f32_32x32x16_bf16 v[64:79], v[48:51], v[196:199], v[64:79]\n"
      "ds_read_b64_tr_b16 v[44:45], %[g11] offset:0\n"
      "ds_read_b64_tr_b16 v[46:47], %[g11] offset:1024\n"
      "v_lshlrev_b32 %[t0], 16, v48\n"
      "v_and_b32 %[t1], %[msk], v48\n"
      "v_add_f32 %[c0], %[c0], %[t0]\n"
      "v_add_f32 %[c0], %[c0], %[t1]\n"
      "v_mfma_f32_32x32x16_bf16 v[80:95], v[52:55], v[196:199], v[80:95]\n"
      "ds_read_b64_tr_b16 v[48:49], %[g12] offset:0\n"
      "ds_read_b64_tr_b16 v[50:51], %[g12] offset:1024\n"
      "v_lshlrev_b32 %[t0], 16, v53\n"
      "v_and_b32 %[t1], %[msk], v53\n"
      "v_add_f32 %[c1], %[c1], %[t0]\n"
      "v_add_f32 %[c1], %[c1], %[t1]\n"
      "s_waitcnt lgkmcnt(10)\n"
      "v_mfma_f32_32x32x16_bf16 v[96:111], v[56:59], v[196:199], v[96:111]\n"
      "ds_read_b64_tr_b16 v[52:53], %[g13] offset:0\n"
      "ds_read_b64_tr_b16 v[54:55], %[g13] offset:1024\n"
      "v_lshlrev_b32 %[t0], 16, v58\n"
      "v_and_b32 %[t1], %[msk], v58\n"
      "v_add_f32 %[c2], %[c2], %[t0]\n"
      "v_add_f32 %[c2], %[c2], %[t1]\n"
      "v_mfma_f32_32x32x16_bf16 v[112:127], v[60:63], v[196:199], v[112:127]\n"
      "ds_read_b64_tr_b16 v[56:57], %[g14] offset:0\n"
      "ds_read_b64_tr_b16 v[58:59], %[g14] offset:1024\n"
      "v_lshlrev_b32 %[t0], 16, v63\n"
      "v_and_b32 %[t1], %[msk], v63\n"
      "v_add_f32 %[c3], %[c3], %[t0]\n"
      "v_add_f32 %[c3], %[c3], %[t1]\n"
      "s_waitcnt lgkmcnt(10)\n"
      "v_mfma_f32_32x32x16_bf16 v[64:79], v[32:35], v[200:203], v[64:79]\n"
      "ds_read_b64_tr_b16 v[60:61], %[g15] offset:0\n"
      "ds_read_b64_tr_b16 v[62:63], %[g15] offset:1024\n"
      "v_lshlrev_b32 %[t0], 16, v32\n"
      "v_and_b32 %[t1], %[msk], v32\n"
      "v_add_f32 %[c0], %[c0], %[t0]\n"
      "v_add_f32 %[c0], %[c0], %[t1]\n"
      "v_mfma_f32_32x32x16_bf16 v[80:95], v[36:39], v[200:203], v[80:95]\n"
      "ds_read_b64_tr_b16 v[32:33], %[g0] offset:16384\n"
      "ds_read_b64_tr_b16 v[34:35], %[g0] offset:17408\n"
      "v_lshlrev_b32 %[t0], 16, v37\n"
      "v_and_b32 %[t1], %[msk], v37\n"
      "v_add_f32 %[c1], %[c1], %[t0]\n"
      "v_add_f32 %[c1], %[c1], %[t1]\n"
      "s_waitcnt lgkmcnt(10)\n"
      "v_mfma_f32_32x32x16_bf16 v[96:111], v[40:43], v[200:203], v[96:111]\n"
      "ds_read_b64_tr_b16 v[36:37], %[g1] offset:16384\n"
      "ds_read_b64_tr_b16 v[38:39], %[g1] offset:17408\n"
      "v_lshlrev_b32 %[t0], 16, v42\n"
      "v_and_b32 %[t1], %[msk], v42\n"
      "v_add_f32 %[c2], %[c2], %[t0]\n"
      "v_add_f32 %[c2], %[c2], %[t1]\n"
      "v_mfma_f32_32x32x16_bf16 v[112:127], v[44:47], v[200:203], v[112:127]\n"
      "ds_read_b64_tr_b16 v[40:41], %[g2] offset:16384\n"
      "ds_read_b64_tr_b16 v[42:43], %[g2] offset:17408\n"
      "v_lshlrev_b32 %[t0], 16, v47\n"
      "v_and_b32 %[t1], %[msk], v47\n"
      "v_add_f32 %[c3], %[c3], %[t0]\n"
      "v_add_f32 %[c3], %[c3], %[t1]\n"
      "s_waitcnt lgkmcnt(10)\n"
      "v_mfma_f32_32x32x16_bf16 v[64:79], v[48:51], v[204:207], v[64:79]\n"
      "ds_read_b64_tr_b16 v[44:45], %[g3] offset:16384\n"
      "ds_read_b64_tr_b16 v[46:47], %[g3] offset:17408\n"
      "v_lshlrev_b32 %[t0], 16, v48\n"
      "v_and_b32 %[t1], %[msk], v48\n"
      "v_add_f32 %[c0], %[c0], %[t0]\n"
      "v_add_f32 %[c0], %[c0], %[t1]\n"
      "v_mfma_f32_32x32x16_bf16 v[80:95], v[52:55], v[204:207], v[80:95]\n"
      "ds_read_b64_tr_b16 v[48:49], %[g4] offset:16384\n"
      "ds_read_b64_tr_b16 v[50:51], %[g4] offset:17408\n"
      "v_lshlrev_b32 %[t0], 16, v53\n"
      "v_and_b32 %[t1], %[msk], v53\n"
      "v_add_f32 %[c1], %[c1], %[t0]\n"
      "v_add_f32 %[c1], %[c1], %[t1]\n"
      "s_waitcnt lgkmcnt(10)\n"
      "v_mfma_f32_32x32x16_bf16 v[96:111], v[56:59], v[204:207], v[96:111]\n"
      "ds_read_b64_tr_b16 v[52:53], %[g5] offset:16384\n"
      "ds_read_b64_tr_b16 v[54:55], %[g5] offset:17408\n"
      "v_lshlrev_b32 %[t0], 16, v58\n"
      "v_and_b32 %[t1], %[msk], v58\n"
      "v_add_f32 %[c2], %[c2], %[t0]\n"
      "v_add_f32 %[c2], %[c2], %[t1]\n"
      "v_mfma_f32_32x32x16_bf16 v[112:127], v[60:63], v[204:207], v[112:127]\n"
      "ds_read_b64_tr_b16 v[56:57], %[g6] offset:16384\n"
      "ds_read_b64_tr_b16 v[58:59], %[g6] offset:17408\n"
      "v_lshlrev_b32 %[t0], 16, v63\n"
      "v_and_b32 %[t1], %[msk], v63\n"
      "v_add_f32 %[c3], %[c3], %[t0]\n"
      "v_add_f32 %[c3], %[c3], %[t1]\n"
      "s_waitcnt lgkmcnt(10)\n"
      "v_mfma_f32_32x32x16_bf16 v[64:79], v[32:35], v[208:211], v[64:79]\n"
      "ds_read_b64_tr_b16 v[60:61], %[g7] offset:16384\n"
      "ds_read_b64_tr_b16 v[62:63], %[g7] offset:17408\n"
      "v_lshlrev_b32 %[t0], 16, v32\n"
      "v_and_b32 %[t1], %[msk], v32\n"
      "v_add_f32 %[c0], %[c0], %[t0]\n"
      "v_add_f32 %[c0], %[c0], %[t1]\n"
      "v_mfma_f32_32x32x16_bf16 v[80:95], v[36:39], v[208:211], v[80:95]\n"
      "ds_read_b64_tr_b16 v[32:33], %[g8] offset:16384\n"
      "ds_read_b64_tr_b16 v[34:35], %[g8] offset:17408\n"
      "v_lshlrev_b32 %[t0], 16, v37\n"
      "v_and_b32 %[t1], %[msk], v37\n"
      "v_add_f32 %[c1], %[c1], %[t0]\n"
      "v_add_f32 %[c1], %[c1], %[t1]\n"
      "s_waitcnt lgkmcnt(10)\n"
      "v_mfma_f32_32x32x16_bf16 v[96:111], v[40:43], v[208:211], v[96:111]\n"
      "ds_read_b64_tr_b16 v[36:37], %[g9] offset:16384\n"
      "ds_read_b64_tr_b16 v[38:39], %[g9] offset:17408\n"
      "v_lshlrev_b32 %[t0], 16, v42\n"
      "v_and_b32 %[t1], %[msk], v42\n"
      "v_add_f32 %[c2], %[c2], %[t0]\n"
      "v_add_f32 %[c2], %[c2], %[t1]\n"
      "v_mfma_f32_32x32x16_bf16 v[112:127], v[44:47], v[208:211], v[112:127]\n"
      "ds_read_b64_tr_b16 v[40:41], %[g10] offset:16384\n"
      "ds_read_b64_tr_b16 v[42:43], %[g10] offset:17408\n"
      "v_lshlrev_b32 %[t0], 16, v47\n"
      "v_and_b32 %[t1], %[msk], v47\n"
      "v_add_f32 %[c3], %[c3], %[t0]\n"
      "v_add_f32 %[c3], %[c3], %[t1]\n"
      "s_waitcnt lgkmcnt(10)\n"
      "v_mfma_f32_32x32x16_bf16 v[64:79], v[48:51], v[212:215], v[64:79]\n"
      "ds_read_b64_tr_b16 v[44:45], %[g11] offset:16384\n"
      "ds_read_b64_tr_b16 v[46:47], %[g11] offset:17408\n"
      "v_lshlrev_b32 %[t0], 16, v48\n"
      "v_and_b32 %[t1], %[msk], v48\n"
      "v_add_f32 %[c0], %[c0], %[t0]\n"
      "v_add_f32 %[c0], %[c0], %[t1]\n"
      "v_mfma_f32_32x32x16_bf16 v[80:95], v[52:55], v[212:215], v[80:95]\n"
      "ds_read_b64_tr_b16 v[48:49], %[g12] offset:16384\n"
      "ds_read_b64_tr_b16 v[50:51], %[g12] offset:17408\n"
      "v_lshlrev_b32 %[t0], 16, v53\n"
      "v_and_b32 %[t1], %[msk], v53\n"
      "v_add_f32 %[c1], %[c1], %[t0]\n"
      "v_add_f32 %[c1], %[c1], %[t1]\n"
      "s_waitcnt lgkmcnt(10)\n"
      "v_mfma_f32_32x32x16_bf16 v[96:111], v[56:59], v[212:215], v[96:111]\n"
      "ds_read_b64_tr_b16 v[52:53], %[g13] offset:16384\n"
      "ds_read_b64_tr_b16 v[54:55], %[g13] offset:17408\n"
      "v_lshlrev_b32 %[t0], 16, v58\n"
      "v_and_b32 %[t1], %[msk], v58\n"
      "v_add_f32 %[c2], %[c2], %[t0]\n"
      "v_add_f32 %[c2], %[c2], %[t1]\n"
      "v_mfma_f32_32x32x16_bf16 v[112:127], v[60:63], v[212:215], v[112:127]\n"
      "ds_read_b64_tr_b16 v[56:57], %[g14] offset:16384\n"
      "ds_read_b64_tr_b16 v[58:59], %[g14] offset:17408\n"
      "v_lshlrev_b32 %[t0], 16, v63\n"
      "v_and_b32 %[t1], %[msk], v63\n"
      "v_add_f32 %[c3], %[c3], %[t0]\n"
      "v_add_f32 %[c3], %[c3], %[t1]\n"
      "s_waitcnt lgkmcnt(10)\n"
      "v_mfma_f32_32x32x16_bf16 v[64:79], v[32:35], v[216:219], v[64:79]\n"
      "ds_read_b64_tr_b16 v[60:61], %[g15] offset:16384\n"
      "ds_read_b64_tr_b16 v[62:63], %[g15] offset:17408\n"
      "v_lshlrev_b32 %[t0], 16, v32\n"
      "v_and_b32 %[t1], %[msk], v32\n"
      "v_add_f32 %[c0], %[c0], %[t0]\n"
      "v_add_f32 %[c0], %[c0], %[t1]\n"
      "v_mfma_f32_32x32x16_bf16 v[80:95], v[36:39], v[216:219], v[80:95]\n"
      "ds_read_b64_tr_b16 v[32:33], %[g0] offset:32768\n"
      "ds_read_b64_tr_b16 v[34:35], %[g0] offset:33792\n"
      "v_lshlrev_b32 %[t0], 16, v37\n"
      "v_and_b32 %[t1], %[msk], v37\n"
      "v_add_f32 %[c1], %[c1], %[t0]\n"
      "v_add_f32 %[c1], %[c1], %[t1]\n"
      "s_waitcnt lgkmcnt(10)\n"
      "v_mfma_f32_32x32x16_bf16 v[96:111], v[40:43], v[216:219], v[96:111]\n"
      "ds_read_b64_tr_b16 v[36:37], %[g1] offset:32768\n"
      "ds_read_b64_tr_b16 v[38:39], %[g1] offset:33792\n"
      "v_lshlrev_b32 %[t0], 16, v42\n"
      "v_and_b32 %[t1], %[msk], v42\n"
      "v_add_f32 %[c2], %[c2], %[t0]\n"
      "v_add_f32 %[c2], %[c2], %[t1]\n"
      "v_mfma_f32_32x32x16_bf16 v[112:127], v[44:47], v[216:219], v[112:127]\n"
      "ds_read_b64_tr_b16 v[40:41], %[g2] offset:32768\n"
      "ds_read_b64_tr_b16 v[42:43], %[g2] offset:33792\n"
      "v_lshlrev_b32 %[t0], 16, v47\n"
      "v_and_b32 %[t1], %[msk], v47\n"
      "v_add_f32 %[c3], %[c3], %[t0]\n"
      "v_add_f32 %[c3], %[c3], %[t1]\n"
      "s_waitcnt lgkmcnt(10)\n"
      "v_mfma_f32_32x32x16_bf16 v[64:79], v[48:51], v[220:223], v[64:79]\n"
      "ds_read_b64_tr_b16 v[44:45], %[g3] offset:32768\n"
      "ds_read_b64_tr_b16 v[46:47], %[g3] offset:33792\n"
      "v_lshlrev_b32 %[t0], 16, v48\n"
      "v_and_b32 %[t1], %[msk], v48\n"
      "v_add_f32 %[c0], %[c0], %[t0]\n"
      "v_add_f32 %[c0], %[c0], %[t1]\n"
      "v_mfma_f32_32x32x16_bf16 v[80:95], v[52:55], v[220:223], v[80:95]\n"
      "ds_read_b64_tr_b16 v[48:49], %[g4] offset:32768\n"
      "ds_read_b64_tr_b16 v[50:51], %[g4] offset:33792\n"
      "v_lshlrev_b32 %[t0], 16, v53\n"
      "v_and_b32 %[t1], %[msk], v53\n"
      "v_add_f32 %[c1], %[c1], %[t0]\n"
      "v_add_f32 %[c1], %[c1], %[t1]\n"
      "s_waitcnt lgkmcnt(10)\n"
      "v_mfma_f32_32x32x16_bf16 v[96:111], v[56:59], v[220:223], v[96:111]\n"
      "ds_read_b64_tr_b16 v[52:53], %[g5] offset:32768\n"
      "ds_read_b64_tr_b16 v[54:55], %[g5] offset:33792\n"
      "v_lshlrev_b32 %[t0], 16, v58\n"
      "v_and_b32 %[t1], %[msk], v58\n"
      "v_add_f32 %[c2], %[c2], %[t0]\n"
      "v_add_f32 %[c2], %[c2], %[t1]\n"
      "v_mfma_f32_32x32x16_bf16 v[112:127], v[60:63], v[220:223], v[112:127]\n"
      "ds_read_b64_tr_b16 v[56:57], %[g6] offset:32768\n"
      "ds_read_b64_tr_b16 v[58:59], %[g6] offset:33792\n"
      "v_lshlrev_b32 %[t0], 16, v63\n"
      "v_and_b32 %[t1], %[msk], v63\n"
      "v_add_f32 %[c3], %[c3], %[t0]\n"
      "v_add_f32 %[c3], %[c3], %[t1]\n"
      "s_waitcnt lgkmcnt(10)\n"
      "v_mfma_f32_32x32x16_bf16 v[64:79], v[32:35], v[224:227], v[64:79]\n"
      "ds_read_b64_tr_b16 v[60:61], %[g7] offset:32768\n"
      "ds_read_b64_tr_b16 v[62:63], %[g7] offset:33792\n"
      "v_lshlrev_b32 %[t0], 16, v32\n"
      "v_and_b32 %[t1], %[msk], v32\n"
      "v_add_f32 %[c0], %[c0], %[t0]\n"
      "v_add_f32 %[c0], %[c0], %[t1]\n"
      "v_mfma_f32_32x32x16_bf16 v[80:95], v[36:39], v[224:227], v[80:95]\n"
      "ds_read_b64_tr_b16 v[32:33], %[g8] offset:32768\n"
      "ds_read_b64_tr_b16 v[34:35], %[g8] offset:33792\n"
      "v_lshlrev_b32 %[t0], 16, v37\n"
      "v_and_b32 %[t1], %[msk], v37\n"
      "v_add_f32 %[c1], %[c1], %[t0]\n"
      "v_add_f32 %[c1], %[c1], %[t1]\n"
      "s_waitcnt lgkmcnt(10)\n"
      "v_mfma_f32_32x32x16_bf16 v[96:111], v[40:43], v[224:227], v[96:111]\n"
      "ds_read_b64_tr_b16 v[36:37], %[g9] offset:32768\n"
      "ds_read_b64_tr_b16 v[38:39], %[g9] offset:33792\n"
      "v_lshlrev_b32 %[t0], 16, v42\n"
      "v_and_b32 %[t1], %[msk], v42\n"
      "v_add_f32 %[c2], %[c2], %[t0]\n"
      "v_add_f32 %[c2], %[c2], %[t1]\n"
      "v_mfma_f32_32x32x16_bf16 v[112:127], v[44:47], v[224:227], v[112:127]\n"
      "ds_read_b64_tr_b16 v[40:41], %[g10] offset:32768\n"
      "ds_read_b64_tr_b16 v[42:43], %[g10] offset:33792\n"
      "v_lshlrev_b32 %[t0], 16, v47\n"
      "v_and_b32 %[t1], %[msk], v47\n"
      "v_add_f32 %[c3], %[c3], %[t0]\n"
      "v_add_f32 %[c3], %[c3], %[t1]\n"
      "s_waitcnt lgkmcnt(10)\n"
      "v_mfma_f32_32x32x16_bf16 v[64:79], v[48:51], v[228:231], v[64:79]\n"
      "ds_read_b64_tr_b16 v[44:45], %[g11] offset:32768\n"
      "ds_read_b64_tr_b16 v[46:47], %[g11] offset:33792\n"
      "v_lshlrev_b32 %[t0], 16, v48\n"
      "v_and_b32 %[t1], %[msk], v48\n"
      "v_add_f32 %[c0], %[c0], %[t0]\n"
      "v_add_f32 %[c0], %[c0], %[t1]\n"
      "v_mfma_f32_32x32x16_bf16 v[80:95], v[52:55], v[228:231], v[80:95]\n"
      "ds_read_b64_tr_b16 v[48:49], %[g12] offset:32768\n"
      "ds_read_b64_tr_b16 v[50:51], %[g12] offset:33792\n"
      "v_lshlrev_b32 %[t0], 16, v53\n"
      "v_and_b32 %[t1], %[msk], v53\n"
      "v_add_f32 %[c1], %[c1], %[t0]\n"
      "v_add_f32 %[c1], %[c1], %[t1]\n"
      "s_waitcnt lgkmcnt(10)\n"
      "v_mfma_f32_32x32x16_bf16 v[96:111], v[56:59], v[228:231], v[96:111]\n"
      "ds_read_b64_tr_b16 v[52:53], %[g13] offset:32768\n"
      "ds_read_b64_tr_b16 v[54:55], %[g13] offset:33792\n"
      "v_lshlrev_b32 %[t0], 16, v58\n"
      "v_and_b32 %[t1], %[msk], v58\n"
      "v_add_f32 %[c2], %[c2], %[t0]\n"
      "v_add_f32 %[c2], %[c2], %[t1]\n"
      "v_mfma_f32_32x32x16_bf16 v[112:127], v[60:63], v[228:231], v[112:127]\n"
      "ds_read_b64_tr_b16 v[56:57], %[g14] offset:32768\n"
      "ds_read_b64_tr_b16 v[58:59], %[g14] offset:33792\n"
      "v_lshlrev_b32 %[t0], 16, v63\n"
      "v_and_b32 %[t1], %[msk], v63\n"
      "v_add_f32 %[c3], %[c3], %[t0]\n"
      "v_add_f32 %[c3], %[c3], %[t1]\n"
      "s_waitcnt lgkmcnt(10)\n"
      "v_mfma_f32_32x32x16_bf16 v[64:79], v[32:35], v[232:235], v[64:79]\n"
      "ds_read_b64_tr_b16 v[60:61], %[g15] offset:32768\n"
      "ds_read_b64_tr_b16 v[62:63], %[g15] offset:33792\n"
      "v_lshlrev_b32 %[t0], 16, v32\n"
      "v_and_b32 %[t1], %[msk], v32\n"
      "v_add_f32 %[c0], %[c0], %[t0]\n"
      "v_add_f32 %[c0], %[c0], %[t1]\n"
      "v_mfma_f32_32x32x16_bf16 v[80:95], v[36:39], v[232:235], v[80:95]\n"
      "ds_read_b64_tr_b16 v[32:33], %[g0] offset:49152\n"
      "ds_read_b64_tr_b16 v[34:35], %[g0] offset:50176\n"
      "v_lshlrev_b32 %[t0], 16, v37\n"
      "v_and_b32 %[t1], %[msk], v37\n"
      "v_add_f32 %[c1], %[c1], %[t0]\n"
      "v_add_f32 %[c1], %[c1], %[t1]\n"
      "s_waitcnt lgkmcnt(10)\n"
      "v_mfma_f32_32x32x16_bf16 v[96:111], v[40:43], v[232:235], v[96:111]\n"
      "ds_read_b64_tr_b16 v[36:37], %[g1] offset:49152\n"
      "ds_read_b64_tr_b16 v[38:39], %[g1] offset:50176\n"
      "v_lshlrev_b32 %[t0], 16, v42\n"
      "v_and_b32 %[t1], %[msk], v42\n"
      "v_add_f32 %[c2], %[c2], %[t0]\n"
      "v_add_f32 %[c2], %[c2], %[t1]\n"
      "v_mfma_f32_32x32x16_bf16 v[112:127], v[44:47], v[232:235], v[112:127]\n"
      "ds_read_b64_tr_b16 v[40:41], %[g2] offset:49152\n"
      "ds_read_b64_tr_b16 v[42:43], %[g2] offset:50176\n"
      "v_lshlrev_b32 %[t0], 16, v47\n"
      "v_and_b32 %[t1], %[msk], v47\n"
      "v_add_f32 %[c3], %[c3], %[t0]\n"
      "v_add_f32 %[c3], %[c3], %[t1]\n"
      "s_waitcnt lgkmcnt(10)\n"
      "v_mfma_f32_32x32x16_bf16 v[64:79], v[48:51], v[236:239], v[64:79]\n"
      "ds_read_b64_tr_b16 v[44:45], %[g3] offset:49152\n"
      "ds_read_b64_tr_b16 v[46:47], %[g3] offset:50176\n"
      "v_lshlrev_b32 %[t0], 16, v48\n"
      "v_and_b32 %[t1], %[msk], v48\n"
      "v_add_f32 %[c0], %[c0], %[t0]\n"
      "v_add_f32 %[c0], %[c0], %[t1]\n"
      "v_mfma_f32_32x32x16_bf16 v[80:95], v[52:55], v[236:239], v[80:95]\n"
      "ds_read_b64_tr_b16 v[48:49], %[g4] offset:49152\n"
      "ds_read_b64_tr_b16 v[50:51], %[g4] offset:50176\n"
      "v_lshlrev_b32 %[t0], 16, v53\n"
      "v_and_b32 %[t1], %[msk], v53\n"
      "v_add_f32 %[c1], %[c1], %[t0]\n"
      "v_add_f32 %[c1], %[c1], %[t1]\n"
      "s_waitcnt lgkmcnt(10)\n"
      "v_mfma_f32_32x32x16_bf16 v[96:111], v[56:59], v[236:239], v[96:111]\n"
      "ds_read_b64_tr_b16 v[52:53], %[g5] offset:49152\n"
      "ds_read_b64_tr_b16 v[54:55], %[g5] offset:50176\n"
      "v_lshlrev_b32 %[t0], 16, v58\n"
      "v_and_b32 %[t1], %[msk], v58\n"
      "v_add_f32 %[c2], %[c2], %[t0]\n"
      "v_add_f32 %[c2], %[c2], %[t1]\n"
      "v_mfma_f32_32x32x16_bf16 v[112:127], v[60:63], v[236:239], v[112:127]\n"
      "ds_read_b64_tr_b16 v[56:57], %[g6] offset:49152\n"
      "ds_read_b64_tr_b16 v[58:59], %[g6] offset:50176\n"
      "v_lshlrev_b32 %[t0], 16, v63\n"
      "v_and_b32 %[t1], %[msk], v63\n"
      "v_add_f32 %[c3], %[c3], %[t0]\n"
      "v_add_f32 %[c3], %[c3], %[t1]\n"
      "s_waitcnt lgkmcnt(10)\n"
      "v_mfma_f32_32x32x16_bf16 v[64:79], v[32:35], v[240:243], v[64:79]\n"
      "ds_read_b64_tr_b16 v[60:61], %[g7] offset:49152\n"
      "ds_read_b64_tr_b16 v[62:63], %[g7] offset:50176\n"
      "v_lshlrev_b32 %[t0], 16, v32\n"
      "v_and_b32 %[t1], %[msk], v32\n"
      "v_add_f32 %[c0], %[c0], %[t0]\n"
      "v_add_f32 %[c0], %[c0], %[t1]\n"
      "v_mfma_f32_32x32x16_bf16 v[80:95], v[36:39], v[240:243], v[80:95]\n"
      "ds_read_b64_tr_b16 v[32:33], %[g8] offset:49152\n"
      "ds_read_b64_tr_b16 v[34:35], %[g8] offset:50176\n"
      "v_lshlrev_b32 %[t0], 16, v37\n"
      "v_and_b32 %[t1], %[msk], v37\n"
      "v_add_f32 %[c1], %[c1], %[t0]\n"
      "v_add_f32 %[c1], %[c1], %[t1]\n"
      "s_waitcnt lgkmcnt(10)\n"
      "v_mfma_f32_32x32x16_bf16 v[96:111], v[40:43], v[240:243], v[96:111]\n"
      "ds_read_b64_tr_b16 v[36:37], %[g9] offset:49152\n"
      "ds_read_b64_tr_b16 v[38:39], %[g9] offset:50176\n"
      "v_lshlrev_b32 %[t0], 16, v42\n"
      "v_and_b32 %[t1], %[msk], v42\n"
      "v_add_f32 %[c2], %[c2], %[t0]\n"
      "v_add_f32 %[c2], %[c2], %[t1]\n"
      "v_mfma_f32_32x32x16_bf16 v[112:127], v[44:47], v[240:243], v[112:127]\n"
      "ds_read_b64_tr_b16 v[40:41], %[g10] offset:49152\n"
      "ds_read_b64_tr_b16 v[42:43], %[g10] offset:50176\n"
      "v_lshlrev_b32 %[t0], 16, v47\n"
      "v_and_b32 %[t1], %[msk], v47\n"
      "v_add_f32 %[c3], %[c3], %[t0]\n"
      "v_add_f32 %[c3], %[c3], %[t1]\n"
      "s_waitcnt lgkmcnt(10)\n"
      "v_mfma_f32_32x32x16_bf16 v[64:79], v[48:51], v[244:247], v[64:79]\n"
      "ds_read_b64_tr_b16 v[44:45], %[g11] offset:49152\n"
      "ds_read_b64_tr_b16 v[46:47], %[g11] offset:50176\n"
      "v_lshlrev_b32 %[t0], 16, v48\n"
      "v_and_b32 %[t1], %[msk], v48\n"
      "v_add_f32 %[c0], %[c0], %[t0]\n"
      "v_add_f32 %[c0], %[c0], %[t1]\n"
      "v_mfma_f32_32x32x16_bf16 v[80:95], v[52:55], v[244:247], v[80:95]\n"
      "ds_read_b64_tr_b16 v[48:49], %[g12] offset:49152\n"
      "ds_read_b64_tr_b16 v[50:51], %[g12] offset:50176\n"
      "v_lshlrev_b32 %[t0], 16, v53\n"
      "v_and_b32 %[t1], %[msk], v53\n"
      "v_add_f32 %[c1], %[c1], %[t0]\n"
      "v_add_f32 %[c1], %[c1], %[t1]\n"
      "s_waitcnt lgkmcnt(10)\n"
      "v_mfma_f32_32x32x16_bf16 v[96:111], v[56:59], v[244:247], v[96:111]\n"
      "ds_read_b64_tr_b16 v[52:53], %[g13] offset:49152\n"
      "ds_read_b64_tr_b16 v[54:55], %[g13] offset:50176\n"
      "v_lshlrev_b32 %[t0], 16, v58\n"
      "v_and_b32 %[t1], %[msk], v58\n"
      "v_add_f32 %[c2], %[c2], %[t0]\n"
      "v_add_f32 %[c2], %[c2], %[t1]\n"
      "v_mfma_f32_32x32x16_bf16 v[112:127], v[60:63], v[244:247], v[112:127]\n"
      "ds_read_b64_tr_b16 v[56:57], %[g14] offset:49152\n"
      "ds_read_b64_tr_b16 v[58:59], %[g14] offset:50176\n"
      "v_lshlrev_b32 %[t0], 16, v63\n"
      "v_and_b32 %[t1], %[msk], v63\n"
      "v_add_f32 %[c3], %[c3], %[t0]\n"
      "v_add_f32 %[c3], %[c3], %[t1]\n"
      "s_waitcnt lgkmcnt(10)\n"
      "v_mfma_f32_32x32x16_bf16 v[64:79], v[32:35], v[248:251], v[64:79]\n"
      "ds_read_b64_tr_b16 v[60:61], %[g15] offset:49152\n"
      "ds_read_b64_tr_b16 v[62:63], %[g15] offset:50176\n"
      "v_lshlrev_b32 %[t0], 16, v32\n"
      "v_and_b32 %[t1], %[msk], v32\n"
      "v_add_f32 %[c0], %[c0], %[t0]\n"
      "v_add_f32 %[c0], %[c0], %[t1]\n"
      "v_mfma_f32_32x32x16_bf16 v[80:95], v[36:39], v[248:251], v[80:95]\n"
      "v_lshlrev_b32 %[t0], 16, v37\n"
      "v_and_b32 %[t1], %[msk], v37\n"
      "v_add_f32 %[c1], %[c1], %[t0]\n"
      "v_add_f32 %[c1], %[c1], %[t1]\n"
      "s_waitcnt lgkmcnt(8)\n"
      "v_mfma_f32_32x32x16_bf16 v[96:111], v[40:43], v[248:251], v[96:111]\n"
      "v_lshlrev_b32 %[t0], 16, v42\n"
      "v_and_b32 %[t1], %[msk], v42\n"
      "v_add_f32 %[c2], %[c2], %[t0]\n"
      "v_add_f32 %[c2], %[c2], %[t1]\n"
      "v_mfma_f32_32x32x16_bf16 v[112:127], v[44:47], v[248:251], v[112:127]\n"
      "v_lshlrev_b32 %[t0], 16, v47\n"
      "v_and_b32 %[t1], %[msk], v47\n"
      "v_add_f32 %[c3], %[c3], %[t0]\n"
      "v_add_f32 %[c3], %[c3], %[t1]\n"
      "s_waitcnt lgkmcnt(4)\n"
      "v_mfma_f32_32x32x16_bf16 v[64:79], v[48:51], v[252:255], v[64:79]\n"
      "v_lshlrev_b32 %[t0], 16, v48\n"
      "v_and_b32 %[t1], %[msk], v48\n"
      "v_add_f32 %[c0], %[c0], %[t0]\n"
      "v_add_f32 %[c0], %[c0], %[t1]\n"
      "v_mfma_f32_32x32x16_bf16 v[80:95], v[52:55], v[252:255], v[80:95]\n"
      "v_lshlrev_b32 %[t0], 16, v53\n"
      "v_and_b32 %[t1], %[msk], v53\n"
      "v_add_f32 %[c1], %[c1], %[t0]\n"
      "v_add_f32 %[c1], %[c1], %[t1]\n"
      "s_waitcnt lgkmcnt(0)\n"
      "v_mfma_f32_32x32x16_bf16 v[96:111], v[56:59], v[252:255], v[96:111]\n"
      "v_lshlrev_b32 %[t0], 16, v58\n"
      "v_and_b32 %[t1], %[msk], v58\n"
      "v_add_f32 %[c2], %[c2], %[t0]\n"
      "v_add_f32 %[c2], %[c2], %[t1]\n"
      "v_mfma_f32_32x32x16_bf16 v[112:127], v[60:63], v[252:255], v[112:127]\n"
      "v_lshlrev_b32 %[t0], 16, v63\n"
      "v_and_b32 %[t1], %[msk], v63\n"
      "v_add_f32 %[c3], %[c3], %[t0]\n"
      "v_add_f32 %[c3], %[c3], %[t1]\n"
      "s_nop 15\n"
        : "+{v[64:79]}"(g0), "+{v[80:95]}"(g1), "+{v[96:111]}"(g2), "+{v[112:127]}"(g3), CSUM, [c3] "+v"(c3) : XIN_HI, G16, [msk] "s"(msk) : GCLOB, "memory");
    RELANE();
    c0 = half_pick(c0, c1); c2 = half_pick(c2, c3);
    lds_wf1(M_CP + ((m >> 1) & 1) * 2048 + (((h ^ m) & 1) * 32 + t) * 16 + m * 4, c0);
    lds_wf1(M_CP + ((~m >> 1) & 1) * 2048 + (((h ^ m) & 1) * 32 + t) * 16 + m * 4, c2);
    unsigned cp[4], ho[4];
#pragma unroll
    for (int j = 0; j < 4; j++) {
      cp[j] = M_CP + ((j ^ m) >> 1) * 2048 + (((j ^ m) & 1) * 32 + t) * 16;
      ho[j] = WS_H + (tile * 112 + 8 * cg + 2 * (j ^ m)) * 1024 + lane * 16;
    }
    const unsigned rv = fresh(rsw[0]);
    ra = __builtin_amdgcn_rcpf(__uint_as_float(rv & 0xffff0000u));
    rowf = __builtin_bit_cast(bf16x8, (u32x4){(rv & 0xffffu) | (rv << 16), rv >> 16, 0u, 0u});
    const float k1s = -2.302208198f * ra, k2s = -0.1029432397f * ra * ra * ra, sgs = __uint_as_float(rv & 0xffff0000u);
    f32x2 k1 = {k1s, k1s}, k2 = {k2s, k2s}, sg = {sgs, sgs};
    asm volatile("s_waitcnt lgkmcnt(0)" : "+{v[38:39]}"(k1), "+{v[40:41]}"(k2), "+{v[42:43]}"(sg), "+{v[44:47]}"(rowf), "+v"(cp[0]), "+v"(cp[1]), "+v"(cp[2]), "+v"(cp[3]),
                 "+v"(ho[0]), "+v"(ho[1]), "+v"(ho[2]), "+v"(ho[3]) :: "memory");
    lds_add(C_CS);
    lds_wait(C_CS, 4);
    asm volatile(
      "ds_read_b128 v[144:147], %[p0]\n"
      "ds_read_b128 v[148:151], %[p1]\n"
      "ds_read_b128 v[152:155], %[p2]\n"
      "ds_read_b128 v[156:159], %[p3]\n"
      "v_cndmask_b32 v129, 0, %[b0], %[low]\n"
      "v_mov_b64 v[130:131], 0\n"
      "v_cndmask_b32 v133, 0, %[b1], %[low]\n"
      "v_mov_b64 v[134:135], 0\n"
      "v_cndmask_b32 v137, 0, %[b2], %[low]\n"
      "v_mov_b64 v[138:139], 0\n"
      "v_cndmask_b32 v141, 0, %[b3], %[low]\n"
      "v_mov_b64 v[142:143], 0\n"
      "s_waitcnt lgkmcnt(0)\n"
      "v_add_f32 v144, v144, v145\n"
      "v_add_f32 v148, v148, v149\n"
      "v_add_f32 v152, v152, v153\n"
      "v_add_f32 v156, v156, v157\n"
      "v_add_f32 v146, v146, v147\n"
      "v_add_f32 v150, v150, v151\n"
      "v_add_f32 v154, v154, v155\n"
      "v_add_f32 v158, v158, v159\n"
      "v_add_f32 v144, v144, v146\n"
      "v_add_f32 v148, v148, v150\n"
      "v_add_f32 v152, v152, v154\n"
      "v_add_f32 v156, v156, v158\n"
      "v_cvt_pk_bf16_f32 v145, v144, v144\n"
      "v_cvt_pk_bf16_f32 v149, v148, v148\n"
      "v_cvt_pk_bf16_f32 v153, v152, v152\n"
      "v_cvt_pk_bf16_f32 v157, v156, v156\n"
      "v_and_b32 v145, %[msk], v145\n"
      "v_and_b32 v149, %[msk], v149\n"
      "v_and_b32 v153, %[msk], v153\n"
      "v_and_b32 v157, %[msk], v157\n"
      "v_sub_f32 v144, v144, v145\n"
      "v_sub_f32 v148, v148, v149\n"
      "v_sub_f32 v152, v152, v153\n"
      "v_sub_f32 v156, v156, v157\n"
      "v_cvt_pk_bf16_f32 v144, v145, v144\n"
      "v_cvt_pk_bf16_f32 v148, v149, v148\n"
      "v_cvt_pk_bf16_f32 v152, v153, v152\n"
      "v_cvt_pk_bf16_f32 v156, v157, v156\n"
      "v_cndmask_b32 v128, 0, v144, %[low]\n"
      "v_cndmask_b32 v132, 0, v148, %[low]\n"
      "v_cndmask_b32 v136, 0, v152, %[low]\n"
      "v_cndmask_b32 v140, 0, v156, %[low]\n"
      "v_mfma_f32_32x32x16_bf16 v[64:79], v[128:131], v[44:47], v[64:79]\n"
      "v_mfma_f32_32x32x16_bf16 v[80:95], v[132:135], v[44:47], v[80:95]\n"
      "v_mfma_f32_32x32x16_bf16 v[96:111], v[136:139], v[44:47], v[96:111]\n"
      "v_mfma_f32_32x32x16_bf16 v[112:127], v[140:143], v[44:47], v[112:127]\n"
      "s_nop 8\n"
      "v_pk_mul_f32 v[160:161], v[64:65], v[64:65]\n"
      "v_pk_mul_f32 v[162:163], v[66:67], v[66:67]\n"
      "v_pk_mul_f32 v[164:165], v[68:69], v[68:69]\n"
      "v_pk_mul_f32 v[166:167], v[70:71], v[70:71]\n"
      "v_pk_fma_f32 v[160:161], v[160:161], %[k2], %[k1]\n"
      "v_pk_fma_f32 v[162:163], v[162:163], %[k2], %[k1]\n"
      "v_pk_fma_f32 v[164:165], v[164:165], %[k2], %[k1]\n"
      "v_pk_fma_f32 v[166:167], v[166:167], %[k2], %[k1]\n"
      "v_pk_mul_f32 v[160:161], v[160:161], v[64:65]\n"
      "v_pk_mul_f32 v[162:163], v[162:163], v[66:67]\n"
      "v_pk_mul_f32 v[164:165], v[164:165], v[68:69]\n"
      "v_pk_mul_f32 v[166:167], v[166:167], v[70:71]\n"
      "v_exp_f32 v160, v160\n"
      "v_exp_f32 v161, v161\n"
      "v_exp_f32 v162, v162\n"
      "v_exp_f32 v163, v163\n"
      "v_exp_f32 v164, v164\n"
      "v_exp_f32 v165, v165\n"
      "v_exp_f32 v166, v166\n"
      "v_exp_f32 v167, v167\n"
      "v_pk_fma_f32 v[160:161], v[160:161], %[sg], %[sg]\n"
      "v_pk_fma_f32 v[162:163], v[162:163], %[sg], %[sg]\n"
      "v_pk_fma_f32 v[164:165], v[164:165], %[sg], %[sg]\n"
      "v_pk_fma_f32 v[166:167], v[166:167], %[sg], %[sg]\n"
      "v_rcp_f32 v160, v160\n"
      "v_rcp_f32 v161, v161\n"
      "v_rcp_f32 v162, v162\n"
      "v_rcp_f32 v163, v163\n"
      "v_rcp_f32 v164, v164\n"
      "v_rcp_f32 v165, v165\n"
      "v_rcp_f32 v166, v166\n"
      "v_rcp_f32 v167, v167\n"
      "v_pk_mul_f32 v[160:161], v[160:161], v[64:65]\n"
      "v_pk_mul_f32 v[162:163], v[162:163], v[66:67]\n"
      "v_pk_mul_f32 v[164:165], v[164:165], v[68:69]\n"
      "v_pk_mul_f32 v[166:167], v[166:167], v[70:71]\n"
      "v_cvt_pk_bf16_f32 v64, v160, v161\n"
      "v_cvt_pk_bf16_f32 v65, v162, v163\n"
      "v_cvt_pk_bf16_f32 v66, v164, v165\n"
      "v_cvt_pk_bf16_f32 v67, v166, v167\n"
      "global_store_dwordx4 %[o0], v[64:67], %[ws] offset:0 sc1\n"
      "v_pk_mul_f32 v[160:161], v[72:73], v[72:73]\n"
      "v_pk_mul_f32 v[162:163], v[74:75], v[74:75]\n"
      "v_pk_mul_f32 v[164:165], v[76:77], v[76:77]\n"
      "v_pk_mul_f32 v[166:167], v[78:79], v[78:79]\n"
      "v_pk_fma_f32 v[160:161], v[160:161], %[k2], %[k1]\n"
      "v_pk_fma_f32 v[162:163], v[162:163], %[k2], %[k1]\n"
      "v_pk_fma_f32 v[164:165], v[164:165], %[k2], %[k1]\n"
      "v_pk_fma_f32 v[166:167], v[166:167], %[k2], %[k1]\n"
      "v_pk_mul_f32 v[160:161], v[160:161], v[72:73]\n"
      "v_pk_mul_f32 v[162:163], v[162:163], v[74:75]\n"
      "v_pk_mul_f32 v[164:165], v[164:165], v[76:77]\n"
      "v_pk_mul_f32 v[166:167], v[166:167], v[78:79]\n"
      "v_exp_f32 v160, v160\n"
      "v_exp_f32 v161, v161\n"
      "v_exp_f32 v162, v162\n"
      "v_exp_f32 v163, v163\n"
      "v_exp_f32 v164, v164\n"
      "v_exp_f32 v165, v165\n"
      "v_exp_f32 v166, v166\n"
      "v_exp_f32 v167, v167\n"
      "v_pk_fma_f32 v[160:161], v[160:161], %[sg], %[sg]\n"
      "v_pk_fma_f32 v[162:163], v[162:163], %[sg], %[sg]\n"
      "v_pk_fma_f32 v[164:165], v[164:165], %[sg], %[sg]\n"
      "v_pk_fma_f32 v[166:167], v[166:167], %[sg], %[sg]\n"
      "v_rcp_f32 v160, v160\n"
      "v_rcp_f32 v161, v161\n"
      "v_rcp_f32 v162, v162\n"
      "v_rcp_f32 v163, v163\n"
      "v_rcp_f32 v164, v164\n"
      "v_rcp_f32 v165, v165\n"
      "v_rcp_f32 v166, v166\n"
      "v_rcp_f32 v167, v167\n"
      "v_pk_mul_f32 v[160:161], v[160:161], v[72:73]\n"
      "v_pk_mul_f32 v[162:163], v[162:163], v[74:75]\n"
      "v_pk_mul_f32 v[164:165], v[164:165], v[76:77]\n"
      "v_pk_mul_f32 v[166:167], v[166:167], v[78:79]\n"
      "v_cvt_pk_bf16_f32 v72, v160, v161\n"
      "v_cvt_pk_bf16_f32 v73, v162, v163\n"
      "v_cvt_pk_bf16_f32 v74, v164, v165\n"
      "v_cvt_pk_bf16_f32 v75, v166, v167\n"
      "global_store_dwordx4 %[o0], v[72:75], %[ws] offset:1024 sc1\n"
      "v_pk_mul_f32 v[160:161], v[80:81], v[80:81]\n"
      "v_pk_mul_f32 v[162:163], v[82:83], v[82:83]\n"
      "v_pk_mul_f32 v[164:165], v[84:85], v[84:85]\n"
      "v_pk_mul_f32 v[166:167], v[86:87], v[86:87]\n"
      "v_pk_fma_f32 v[160:161], v[160:161], %[k2], %[k1]\n"
      "v_pk_fma_f32 v[162:163], v[162:163], %[k2], %[k1]\n"
      "v_pk_fma_f32 v[164:165], v[164:165], %[k2], %[k1]\n"
      "v_pk_fma_f32 v[166:167], v[166:167], %[k2], %[k1]\n"
      "v_pk_mul_f32 v[160:161], v[160:161], v[80:81]\n"
      "v_pk_mul_f32 v[162:163], v[162:163], v[82:83]\n"
      "v_pk_mul_f32 v[164:165], v[164:165], v[84:85]\n"
      "v_pk_mul_f32 v[166:167], v[166:167], v[86:87]\n"
      "v_exp_f32 v160, v160\n"
      "v_exp_f32 v161, v161\n"
      "v_exp_f32 v162, v162\n"
      "v_exp_f32 v163, v163\n"
      "v_exp_f32 v164, v164\n"
      "v_exp_f32 v165, v165\n"
      "v_exp_f32 v166, v166\n"
      "v_exp_f32 v167, v167\n"
      "v_pk_fma_f32 v[160:161], v[160:161], %[sg], %[sg]\n"
      "v_pk_fma_f32 v[162:163], v[162:163], %[sg], %[sg]\n"
      "v_pk_fma_f32 v[164:165], v[164:165], %[sg], %[sg]\n"
      "v_pk_fma_f32 v[166:167], v[166:167], %[sg], %[sg]\n"
      "v_rcp_f32 v160, v160\n"
      "v_rcp_f32 v161, v161\n"
      "v_rcp_f32 v162, v162\n"
      "v_rcp_f32 v163, v163\n"
      "v_rcp_f32 v164, v164\n"
      "v_rcp_f32 v165, v165\n"
      "v_rcp_f32 v166, v166\n"
      "v_rcp_f32 v167, v167\n"
      "v_pk_mul_f32 v[160:161], v[160:161], v[80:81]\n"
      "v_pk_mul_f32 v[162:163], v[162:163], v[82:83]\n"
      "v_pk_mul_f32 v[164:165], v[164:165], v[84:85]\n"
      "v_pk_mul_f32 v[166:167], v[166:167], v[86:87]\n"
      "v_cvt_pk_bf16_f32 v80, v160, v161\n"
      "v_cvt_pk_bf16_f32 v81, v162, v163\n"
      "v_cvt_pk_bf16_f32 v82, v164, v165\n"
      "v_cvt_pk_bf16_f32 v83, v166, v167\n"
      "global_store_dwordx4 %[o1], v[80:83], %[ws] offset:0 sc1\n"
      "v_pk_mul_f32 v[160:161], v[88:89], v[88:89]\n"
      "v_pk_mul_f32 v[162:163], v[90:91], v[90:91]\n"
      "v_pk_mul_f32 v[164:165], v[92:93], v[92:93]\n"
      "v_pk_mul_f32 v[166:167], v[94:95], v[94:95]\n"
      "v_pk_fma_f32 v[160:161], v[160:161], %[k2], %[k1]\n"
      "v_pk_fma_f32 v[162:163], v[162:163], %[k2], %[k1]\n"
      "v_pk_fma_f32 v[164:165], v[164:165], %[k2], %[k1]\n"
      "v_pk_fma_f32 v[166:167], v[166:167], %[k2], %[k1]\n"
      "v_pk_mul_f32 v[160:161], v[160:161], v[88:89]\n"
      "v_pk_mul_f32 v[162:163], v[162:163], v[90:91]\n"
      "v_pk_mul_f32 v[164:165], v[164:165], v[92:93]\n"
      "v_pk_mul_f32 v[166:167], v[166:167], v[94:95]\n"
      "v_exp_f32 v160, v160\n"
      "v_exp_f32 v161, v161\n"
      "v_exp_f32 v162, v162\n"
      "v_exp_f32 v163, v163\n"
      "v_exp_f32 v164, v164\n"
      "v_exp_f32 v165, v165\n"
      "v_exp_f32 v166, v166\n"
      "v_exp_f32 v167, v167\n"
      "v_pk_fma_f32 v[160:161], v[160:161], %[sg], %[sg]\n"
      "v_pk_fma_f32 v[162:163], v[162:163], %[sg], %[sg]\n"
      "v_pk_fma_f32 v[164:165], v[164:165], %[sg], %[sg]\n"
      "v_pk_fma_f32 v[166:167], v[166:167], %[sg], %[sg]\n"
      "v_rcp_f32 v160, v160\n"
      "v_rcp_f32 v161, v161\n"
      "v_rcp_f32 v162, v162\n"
      "v_rcp_f32 v163, v163\n"
      "v_rcp_f32 v164, v164\n"
      "v_rcp_f32 v165, v165\n"
      "v_rcp_f32 v166, v166\n"
      "v_rcp_f32 v167, v167\n"
      "v_pk_mul_f32 v[160:161], v[160:161], v[88:89]\n"
      "v_pk_mul_f32 v[162:163], v[162:163], v[90:91]\n"
      "v_pk_mul_f32 v[164:165], v[164:165], v[92:93]\n"
      "v_pk_mul_f32 v[166:167], v[166:167], v[94:95]\n"
      "v_cvt_pk_bf16_f32 v88, v160, v161\n"
      "v_cvt_pk_bf16_f32 v89, v162, v163\n"
      "v_cvt_pk_bf16_f32 v90, v164, v165\n"
      "v_cvt_pk_bf16_f32 v91, v166, v167\n"
      "global_store_dwordx4 %[o1], v[88:91], %[ws] offset:1024 sc1\n"
      "v_pk_mul_f32 v[160:161], v[96:97], v[96:97]\n"
      "v_pk_mul_f32 v[162:163], v[98:99], v[98:99]\n"
      "v_pk_mul_f32 v[164:165], v[100:101], v[100:101]\n"
      "v_pk_mul_f32 v[166:167], v[102:103], v[102:103]\n"
      "v_pk_fma_f32 v[160:161], v[160:161], %[k2], %[k1]\n"
      "v_pk_fma_f32 v[162:163], v[162:163], %[k2], %[k1]\n"
      "v_pk_fma_f32 v[164:165], v[164:165], %[k2], %[k1]\n"
      "v_pk_fma_f32 v[166:167], v[166:167], %[k2], %[k1]\n"
      "v_pk_mul_f32 v[160:161], v[160:161], v[96:97]\n"
      "v_pk_mul_f32 v[162:163], v[162:163], v[98:99]\n"
      "v_pk_mul_f32 v[164:165], v[164:165], v[100:101]\n"
      "v_pk_mul_f32 v[166:167], v[166:167], v[102:103]\n"
      "v_exp_f32 v160, v160\n"
      "v_exp_f32 v161, v161\n"
      "v_exp_f32 v162, v162\n"
      "v_exp_f32 v163, v163\n"
      "v_exp_f32 v164, v164\n"
      "v_exp_f32 v165, v165\n"
      "v_exp_f32 v166, v166\n"
      "v_exp_f32 v167, v167\n"
      "v_pk_fma_f32 v[160:161], v[160:161], %[sg], %[sg]\n"
      "v_pk_fma_f32 v[162:163], v[162:163], %[sg], %[sg]\n"
      "v_pk_fma_f32 v[164:165], v[164:165], %[sg], %[sg]\n"
      "v_pk_fma_f32 v[166:167], v[166:167], %[sg], %[sg]\n"
      "v_rcp_f32 v160, v160\n"
      "v_rcp_f32 v161, v161\n"
      "v_rcp_f32 v162, v162\n"
      "v_rcp_f32 v163, v163\n"
      "v_rcp_f32 v164, v164\n"
      "v_rcp_f32 v165, v165\n"
      "v_rcp_f32 v166, v166\n"
      "v_rcp_f32 v167, v167\n"
      "v_pk_mul_f32 v[160:161], v[160:161], v[96:97]\n"
      "v_pk_mul_f32 v[162:163], v[162:163], v[98:99]\n"
      "v_pk_mul_f32 v[164:165], v[164:165], v[100:101]\n"
      "v_pk_mul_f32 v[166:167], v[166:167], v[102:103]\n"
      "v_cvt_pk_bf16_f32 v96, v160, v161\n"
      "v_cvt_pk_bf16_f32 v97, v162, v163\n"
      "v_cvt_pk_bf16_f32 v98, v164, v165\n"
      "v_cvt_pk_bf16_f32 v99, v166, v167\n"
      "global_store_dwordx4 %[o2], v[96:99], %[ws] offset:0 sc1\n"
      "v_pk_mul_f32 v[160:161], v[104:105], v[104:105]\n"
      "v_pk_mul_f32 v[162:163], v[106:107], v[106:107]\n"
      "v_pk_mul_f32 v[164:165], v[108:109], v[108:109]\n"
      "v_pk_mul_f32 v[166:167], v[110:111], v[110:111]\n"
      "v_pk_fma_f32 v[160:161], v[160:161], %[k2], %[k1]\n"
      "v_pk_fma_f32 v[162:163], v[162:163], %[k2], %[k1]\n"
      "v_pk_fma_f32 v[164:165], v[164:165], %[k2], %[k1]\n"
      "v_pk_fma_f32 v[166:167], v[166:167], %[k2], %[k1]\n"
      "v_pk_mul_f32 v[160:161], v[160:161], v[104:105]\n"
      "v_pk_mul_f32 v[162:163], v[162:163], v[106:107]\n"
      "v_pk_mul_f32 v[164:165], v[164:165], v[108:109]\n"
      "v_pk_mul_f32 v[166:167], v[166:167], v[110:111]\n"
      "v_exp_f32 v160, v160\n"
      "v_exp_f32 v161, v161\n"
      "v_exp_f32 v162, v162\n"
      "v_exp_f32 v163, v163\n"
      "v_exp_f32 v164, v164\n"
      "v_exp_f32 v165, v165\n"
      "v_exp_f32 v166, v166\n"
      "v_exp_f32 v167, v167\n"
      "v_pk_fma_f32 v[160:161], v[160:161], %[sg], %[sg]\n"
      "v_pk_fma_f32 v[162:163], v[162:163], %[sg], %[sg]\n"
      "v_pk_fma_f32 v[164:165], v[164:165], %[sg], %[sg]\n"
      "v_pk_fma_f32 v[166:167], v[166:167], %[sg], %[sg]\n"
      "v_rcp_f32 v160, v160\n"
      "v_rcp_f32 v161, v161\n"
      "v_rcp_f32 v162, v162\n"
      "v_rcp_f32 v163, v163\n"
      "v_rcp_f32 v164, v164\n"
      "v_rcp_f32 v165, v165\n"
      "v_rcp_f32 v166, v166\n"
      "v_rcp_f32 v167, v167\n"
      "v_pk_mul_f32 v[160:161], v[160:161], v[104:105]\n"
      "v_pk_mul_f32 v[162:163], v[162:163], v[106:107]\n"
      "v_pk_mul_f32 v[164:165], v[164:165], v[108:109]\n"
      "v_pk_mul_f32 v[166:167], v[166:167], v[110:111]\n"
      "v_cvt_pk_bf16_f32 v104, v160, v161\n"
      "v_cvt_pk_bf16_f32 v105, v162, v163\n"
      "v_cvt_pk_bf16_f32 v106, v164, v165\n"
      "v_cvt_pk_bf16_f32 v107, v166, v167\n"
      "global_store_dwordx4 %[o2], v[104:107], %[ws] offset:1024 sc1\n"
      "v_pk_mul_f32 v[160:161], v[112:113], v[112:113]\n"
      "v_pk_mul_f32 v[162:163], v[114:115], v[114:115]\n"
      "v_pk_mul_f32 v[164:165], v[116:117], v[116:117]\n"
      "v_pk_mul_f32 v[166:167], v[118:119], v[118:119]\n"
      "v_pk_fma_f32 v[160:161], v[160:161], %[k2], %[k1]\n"
      "v_pk_fma_f32 v[162:163], v[162:163], %[k2], %[k1]\n"
      "v_pk_fma_f32 v[164:165], v[164:165], %[k2], %[k1]\n"
      "v_pk_fma_f32 v[166:167], v[166:167], %[k2], %[k1]\n"
      "v_pk_mul_f32 v[160:161], v[160:161], v[112:113]\n"
      "v_pk_mul_f32 v[162:163], v[162:163], v[114:115]\n"
      "v_pk_mul_f32 v[164:165], v[164:165], v[116:117]\n"
      "v_pk_mul_f32 v[166:167], v[166:167], v[118:119]\n"
      "v_exp_f32 v160, v160\n"
      "v_exp_f32 v161, v161\n"
      "v_exp_f32 v162, v162\n"
      "v_exp_f32 v163, v163\n"
      "v_exp_f32 v164, v164\n"
      "v_exp_f32 v165, v165\n"
      "v_exp_f32 v166, v166\n"
      "v_exp_f32 v167, v167\n"
      "v_pk_fma_f32 v[160:161], v[160:161], %[sg], %[sg]\n"
      "v_pk_fma_f32 v[162:163], v[162:163], %[sg], %[sg]\n"
      "v_pk_fma_f32 v[164:165], v[164:165], %[sg], %[sg]\n"
      "v_pk_fma_f32 v[166:167], v[166:167], %[sg], %[sg]\n"
      "v_rcp_f32 v160, v160\n"
      "v_rcp_f32 v161, v161\n"
      "v_rcp_f32 v162, v162\n"
      "v_rcp_f32 v163, v163\n"
      "v_rcp_f32 v164, v164\n"
      "v_rcp_f32 v165, v165\n"
      "v_rcp_f32 v166, v166\n"
      "v_rcp_f32 v167, v167\n"
      "v_pk_mul_f32 v[160:161], v[160:161], v[112:113]\n"
      "v_pk_mul_f32 v[162:163], v[162:163], v[114:115]\n"
      "v_pk_mul_f32 v[164:165], v[164:165], v[116:117]\n"
      "v_pk_mul_f32 v[166:167], v[166:167], v[118:119]\n"
      "v_cvt_pk_bf16_f32 v112, v160, v161\n"
      "v_cvt_pk_bf16_f32 v113, v162, v163\n"
      "v_cvt_pk_bf16_f32 v114, v164, v165\n"
      "v_cvt_pk_bf16_f32 v115, v166, v167\n"
      "global_store_dwordx4 %[o3], v[112:115], %[ws] offset:0 sc1\n"
      "v_pk_mul_f32 v[160:161], v[120:121], v[120:121]\n"
      "v_pk_mul_f32 v[162:163], v[122:123], v[122:123]\n"
      "v_pk_mul_f32 v[164:165], v[124:125], v[124:125]\n"
      "v_pk_mul_f32 v[166:167], v[126:127], v[126:127]\n"
      "v_pk_fma_f32 v[160:161], v[160:161], %[k2], %[k1]\n"
      "v_pk_fma_f32 v[162:163], v[162:163], %[k2], %[k1]\n"
      "v_pk_fma_f32 v[164:165], v[164:165], %[k2], %[k1]\n"
      "v_pk_fma_f32 v[166:167], v[166:167], %[k2], %[k1]\n"
      "v_pk_mul_f32 v[160:161], v[160:161], v[120:121]\n"
      "v_pk_mul_f32 v[162:163], v[162:163], v[122:123]\n"
      "v_pk_mul_f32 v[164:165], v[164:165], v[124:125]\n"
      "v_pk_mul_f32 v[166:167], v[166:167], v[126:127]\n"
      "v_exp_f32 v160, v160\n"
      "v_exp_f32 v161, v161\n"
      "v_exp_f32 v162, v162\n"
      "v_exp_f32 v163, v163\n"
      "v_exp_f32 v164, v164\n"
      "v_exp_f32 v165, v165\n"
      "v_exp_f32 v166, v166\n"
      "v_exp_f32 v167, v167\n"
      "v_pk_fma_f32 v[160:161], v[160:161], %[sg], %[sg]\n"
      "v_pk_fma_f32 v[162:163], v[162:163], %[sg], %[sg]\n"
      "v_pk_fma_f32 v[164:165], v[164:165], %[sg], %[sg]\n"
      "v_pk_fma_f32 v[166:167], v[166:167], %[sg], %[sg]\n"
      "v_rcp_f32 v160, v160\n"
      "v_rcp_f32 v161, v161\n"
      "v_rcp_f32 v162, v162\n"
      "v_rcp_f32 v163, v163\n"
      "v_rcp_f32 v164, v164\n"
      "v_rcp_f32 v165, v165\n"
      "v_rcp_f32 v166, v166\n"
      "v_rcp_f32 v167, v167\n"
      "v_pk_mul_f32 v[160:161], v[160:161], v[120:121]\n"
      "v_pk_mul_f32 v[162:163], v[162:163], v[122:123]\n"
      "v_pk_mul_f32 v[164:165], v[164:165], v[124:125]\n"
      "v_pk_mul_f32 v[166:167], v[166:167], v[126:127]\n"
      "v_cvt_pk_bf16_f32 v120, v160, v161\n"
      "v_cvt_pk_bf16_f32 v121, v162, v163\n"
      "v_cvt_pk_bf16_f32 v122, v164, v165\n"
      "v_cvt_pk_bf16_f32 v123, v166, v167\n"
      "global_store_dwordx4 %[o3], v[120:123], %[ws] offset:1024 sc1\n"
      "s_nop 1\n"
        : "+{v[64:79]}"(g0), "+{v[80:95]}"(g1), "+{v[96:111]}"(g2), "+{v[112:127]}"(g3)
        : "{v[44:47]}"(rowf), [k1] "{v[38:39]}"(k1), [k2] "{v[40:41]}"(k2), [sg] "{v[42:43]}"(sg), [p0] "v"(cp[0]), [p1] "v"(cp[1]), [p2] "v"(cp[2]), [p3] "v"(cp[3]),
          [o0] "v"(ho[0]), [o1] "v"(ho[1]), [o2] "v"(ho[2]), [o3] "v"(ho[3]), [b0] "v"(bias[0]), [b1] "v"(bias[1]), [b2] "v"(bias[2]), [b3] "v"(bias[3]),
          [ws] "s"(ws), [msk] "s"(msk), [low] "s"(0xffffffffull)
        : "v128", "v129", "v130", "v131", "v132", "v133", "v134", "v135", "v136", "v137", "v138", "v139", "v140", "v141", "v142", "v143", "v144", "v145", "v146", "v147", "v148", "v149", "v150", "v151", "v152", "v153", "v154", "v155", "v156", "v157", "v158", "v159", "v160", "v161", "v162", "v163", "v164", "v165", "v166", "v167", "memory");
  } else {
    lds_wait(C_FW1, 4);
    lds_add(C_F1);
  }
  {
    RELANE();
    const int mr = m >> 1, mc = m & 1;
    const char* hp = ws + WS_H + (size_t)(2 * rt * 112 + 28 * m) * 1024;
    const char* wp = (const char*)w2 + (size_t)(28 * m * 16) * 1024 + ct * 128;
    const unsigned wvo = (lane >> 3) * 1024 + (lane & 7) * 16, hvo = lane * 16 + mr * (112 * 1024), hvo2 = lane * 16 + (mr ^ 1) * (112 * 1024);
    const unsigned ring = RING + m * 6144;
    const unsigned tr0 = ring + (4 * h + trq) * 128 + trc + mc * 64, tr1 = tr0 ^ 64;
    const char* wop = (const char*)wo + (size_t)(8 * m * 16) * 1024 + ct * 128;
    f32x16 wf[4];
#define WOFETCH(r) _Pragma("unroll") for (int s = 3 * (r); s < 3 * (r) + 3 && s < 8; s++) { dma16(wop + s * 16384, wvo, ring + (s - 3 * (r)) * 2048); dma16(wop + s * 16384 + 8192, wvo, ring + (s - 3 * (r)) * 2048 + 1024); }
    lds_wait(C_KV1, 4);
    WOFETCH(0);
    VMCNT(6);
    if (ffn) arrive(C_H, ws, WS_FH + (rb * 16 + cg) * 8, tag);
    u32x2 xq[4], bq[4];
#pragma unroll
    for (int g = 0; g < 4; g++) {
      asm volatile("global_load_dwordx2 %0, %1, %2" : "=v"(xq[g]) : "v"((unsigned)((rt * 64 + mr * 32 + t) * 512 + ct * 64 + mc * 32 + 8 * g + 4 * h) * 2u), "s"(x) : "memory");
      asm volatile("global_load_dwordx2 %0, %1, %2" : "=v"(bq[g]) : "v"((unsigned)(ct * 64 + mc * 32 + 8 * g + 4 * h) * 2u), "s"(b2) : "memory");
    }
#pragma unroll
    for (int i = 0; i < 16; i++) { const unsigned o = (m + (i & 1) * 4) * 1024; dma16_sc(kvp + KVSRC(8 + (i >> 1)) + o, lane * 16, L_KV + (8 + (i >> 1)) * 8192 + o); }
    asm volatile("s_waitcnt vmcnt(16)" : "+v"(xq[0]), "+v"(xq[1]), "+v"(xq[2]), "+v"(xq[3]), "+v"(bq[0]), "+v"(bq[1]), "+v"(bq[2]), "+v"(bq[3]) :: "memory");
    asm volatile(
      "ds_read_b64_tr_b16 v[128:129], %[t0] offset:0\n"
      "ds_read_b64_tr_b16 v[130:131], %[t0] offset:1024\n"
      "ds_read_b64_tr_b16 v[132:133], %[t1] offset:0\n"
      "ds_read_b64_tr_b16 v[134:135], %[t1] offset:1024\n"
      "ds_read_b64_tr_b16 v[136:137], %[t0] offset:2048\n"
      "ds_read_b64_tr_b16 v[138:139], %[t0] offset:3072\n"
      "ds_read_b64_tr_b16 v[140:141], %[t1] offset:2048\n"
      "ds_read_b64_tr_b16 v[142:143], %[t1] offset:3072\n"
      "ds_read_b64_tr_b16 v[144:145], %[t0] offset:4096\n"
      "ds_read_b64_tr_b16 v[146:147], %[t0] offset:5120\n"
      "ds_read_b64_tr_b16 v[148:149], %[t1] offset:4096\n"
      "ds_read_b64_tr_b16 v[150:151], %[t1] offset:5120\n"
      "s_waitcnt lgkmcnt(0)\n"
        : "={v[128:143]}"(wf[0]), "={v[144:159]}"(wf[1]) : [t0] "v"(tr0), [t1] "v"(tr1) : "memory");
    WOFETCH(1);
    f32x16 acc[4] = {};
#pragma unroll
    for (int g = 0; g < 4; g++)
#pragma unroll
      for (int j = 0; j < 2; j++) {
        acc[0][4 * g + 2 * j] = __uint_as_float(xq[g][j] << 16) + __uint_as_float(bq[g][j] << 16);
        acc[0][4 * g + 2 * j + 1] = __uint_as_float(xq[g][j] & 0xffff0000u) + __uint_as_float(bq[g][j] & 0xffff0000u);
      }
    PIN4(acc);
    VMCNT(0);
    lds_add(C_KV2);
    asm volatile(
      "ds_read_b64_tr_b16 v[152:153], %[t0] offset:0\n"
      "ds_read_b64_tr_b16 v[154:155], %[t0] offset:1024\n"
      "ds_read_b64_tr_b16 v[156:157], %[t1] offset:0\n"
      "ds_read_b64_tr_b16 v[158:159], %[t1] offset:1024\n"
      "ds_read_b64_tr_b16 v[160:161], %[t0] offset:2048\n"
      "ds_read_b64_tr_b16 v[162:163], %[t0] offset:3072\n"
      "ds_read_b64_tr_b16 v[164:165], %[t1] offset:2048\n"
      "ds_read_b64_tr_b16 v[166:167], %[t1] offset:3072\n"
      "ds_read_b64_tr_b16 v[168:169], %[t0] offset:4096\n"
      "ds_read_b64_tr_b16 v[170:171], %[t0] offset:5120\n"
      "ds_read_b64_tr_b16 v[172:173], %[t1] offset:4096\n"
      "ds_read_b64_tr_b16 v[174:175], %[t1] offset:5120\n"
      "s_waitcnt lgkmcnt(0)\n"
        : "+{v[144:159]}"(wf[1]), "={v[160:175]}"(wf[2]) : [t0] "v"(tr0), [t1] "v"(tr1) : "memory");
    WOFETCH(2);
    VMCNT(0);
    asm volatile(
      "ds_read_b64_tr_b16 v[176:177], %[t0] offset:0\n"
      "ds_read_b64_tr_b16 v[178:179], %[t0] offset:1024\n"
      "ds_read_b64_tr_b16 v[180:181], %[t1] offset:0\n"
      "ds_read_b64_tr_b16 v[182:183], %[t1] offset:1024\n"
      "ds_read_b64_tr_b16 v[184:185], %[t0] offset:2048\n"
      "ds_read_b64_tr_b16 v[186:187], %[t0] offset:3072\n"
      "ds_read_b64_tr_b16 v[188:189], %[t1] offset:2048\n"
      "ds_read_b64_tr_b16 v[190:191], %[t1] offset:3072\n"
      "s_waitcnt lgkmcnt(0)\n"
        : "={v[176:191]}"(wf[3]) : [t0] "v"(tr0), [t1] "v"(tr1) : "memory");
    u32x4 hb[3][2];
    const int rot = (7 * (idx & 3) + 3 * ct) % 28;
#define KROT(s) ((s) + rot >= 28 ? (s) + rot - 28 : (s) + rot)
#define FETCH(s, u) do { const int k_ = KROT(s); dma16(wp + k_ * 16384, wvo, ring + (u) * 2048); dma16(wp + k_ * 16384 + 8192, wvo, ring + (u) * 2048 + 1024); \
                         gload_sc(hb[u][0], hp + k_ * 1024, hvo); gload_sc(hb[u][1], hp + k_ * 1024, hvo2); } while (0)
#define STEP(u) do { const bf16x8 wa = lds_tr2(tr0 + (u) * 2048, 1024), wc = lds_tr2(tr1 + (u) * 2048, 1024); \
                     const bf16x8 h0 = __builtin_bit_cast(bf16x8, hb[u][0]), h1 = __builtin_bit_cast(bf16x8, hb[u][1]); \
                     acc[0] = MFMA(wa, h0, acc[0]); GAP(acc[0]); acc[1] = MFMA(wc, h0, acc[1]); GAP(acc[1]); acc[2] = MFMA(wa, h1, acc[2]); GAP(acc[2]); \
                     acc[3] = MFMA(wc, h1, acc[3]); PIN4(acc); LGKM0(); } while (0)
    if (m == 0) {
      const int px = t >> 2, pi = (t & 3) + 4 * (rt >> 2);
      wait_flags(ws, lane < 14 ? WS_FH + ((rt >> 1) * 16 + lane) * 8 : lane < 32 ? 0xffffffffu
                     : WS_FQ + ((8 * (px & 1) + (pi >> 2)) * 16 + 4 * (px >> 1) + (pi & 3)) * 8, tag);
      lds_add(C_FH);
    } else lds_wait(C_FH, 1);
    __builtin_amdgcn_s_setprio(0);
    FETCH(0, 0); FETCH(1, 1); FETCH(2, 2);
#pragma nounroll
    for (int it = 0; it < 9; it++) {
#pragma unroll
      for (int u = 0; u < 3; u++) {
        const int s = it * 3 + u + 3;
        asm volatile("s_waitcnt vmcnt(8)" : "+v"(hb[u][0]), "+v"(hb[u][1]) :: "memory");
        STEP(u);
        FETCH(s < 28 ? s : 27, u);
      }
    }
    asm volatile("s_waitcnt vmcnt(0)" : "+v"(hb[0][0]), "+v"(hb[0][1]), "+v"(hb[1][0]), "+v"(hb[1][1]), "+v"(hb[2][0]), "+v"(hb[2][1]) :: "memory");
    STEP(0);
    asm volatile("s_barrier" : "+{v[64:79]}"(acc[0]), "+{v[80:95]}"(acc[1]), "+{v[96:111]}"(acc[2]), "+{v[112:127]}"(acc[3]) :: "memory");
    {
      const char* op = ws + WS_O + (size_t)(2 * rt * 32 + 8 * m) * 1024;
      asm volatile(
      "s_mov_b32 s40, 15\n"
      "s_mov_b32 s41, 0x8000\n"
      "s_mov_b32 s43, 0\n"
      "s_sleep 8\n"
      "1:\n"
      "s_mov_b32 s42, s40\n"
      "s_bitcmp1_b32 s40, 0\n"
      "s_cbranch_scc0 10f\n"
      "global_load_dwordx4 v[192:195], %[o0], %[op0] offset:0 sc1\n"
      "global_load_dwordx4 v[196:199], %[o1], %[op0] offset:0 sc1\n"
      "global_load_dwordx4 v[200:203], %[o0], %[op0] offset:1024 sc1\n"
      "global_load_dwordx4 v[204:207], %[o1], %[op0] offset:1024 sc1\n"
      "10:\n"
      "s_bitcmp1_b32 s40, 1\n"
      "s_cbranch_scc0 11f\n"
      "global_load_dwordx4 v[208:211], %[o0], %[op0] offset:2048 sc1\n"
      "global_load_dwordx4 v[212:215], %[o1], %[op0] offset:2048 sc1\n"
      "global_load_dwordx4 v[216:219], %[o0], %[op0] offset:3072 sc1\n"
      "global_load_dwordx4 v[220:223], %[o1], %[op0] offset:3072 sc1\n"
      "11:\n"
      "s_bitcmp1_b32 s40, 2\n"
      "s_cbranch_scc0 12f\n"
      "global_load_dwordx4 v[224:227], %[o0], %[op1] offset:0 sc1\n"
      "global_load_dwordx4 v[228:231], %[o1], %[op1] offset:0 sc1\n"
      "global_load_dwordx4 v[232:235], %[o0], %[op1] offset:1024 sc1\n"
      "global_load_dwordx4 v[236:239], %[o1], %[op1] offset:1024 sc1\n"
      "12:\n"
      "s_bitcmp1_b32 s40, 3\n"
      "s_cbranch_scc0 13f\n"
      "global_load_dwordx4 v[240:243], %[o0], %[op1] offset:2048 sc1\n"
      "global_load_dwordx4 v[244:247], %[o1], %[op1] offset:2048 sc1\n"
      "global_load_dwordx4 v[248:251], %[o0], %[op1] offset:3072 sc1\n"
      "global_load_dwordx4 v[252:255], %[o1], %[op1] offset:3072 sc1\n"
      "13:\n"
      "s_cmp_eq_u32 s42, 15\n"
      "s_cbranch_scc1 2f\n"
      "s_waitcnt vmcnt(0)\n"
      "2:\n"
      "s_bitcmp1_b32 s40, 0\n"
      "s_cbranch_scc0 30f\n"
      "s_waitcnt vmcnt(12)\n"
      "v_max3_u32 %[t0], v192, v193, v194\n"
      "v_max3_u32 %[t1], v195, v196, v197\n"
      "v_max3_u32 %[t0], v198, v199, %[t0]\n"
      "v_max3_u32 %[t1], v200, v201, %[t1]\n"
      "v_max3_u32 %[t0], v202, v203, %[t0]\n"
      "v_max3_u32 %[t1], v204, v205, %[t1]\n"
      "v_max3_u32 %[t0], v206, v207, %[t0]\n"
      "v_max_u32 %[t0], %[t0], %[t1]\n"
      "v_cmp_eq_u32 vcc, -1, %[t0]\n"
      "s_cbranch_vccnz 20f\n"
      "s_bitset0_b32 s40, 0\n"
      "30:\n"
      "s_cmp_lg_u32 s43, 0\n"
      "s_cbranch_scc1 20f\n"
      "v_mfma_f32_32x32x16_bf16 v[64:79], v[128:131], v[192:195], v[64:79]\n"
      "v_mfma_f32_32x32x16_bf16 v[80:95], v[132:135], v[192:195], v[80:95]\n"
      "v_mfma_f32_32x32x16_bf16 v[96:111], v[128:131], v[196:199], v[96:111]\n"
      "v_mfma_f32_32x32x16_bf16 v[112:127], v[132:135], v[196:199], v[112:127]\n"
      "v_mfma_f32_32x32x16_bf16 v[64:79], v[136:139], v[200:203], v[64:79]\n"
      "v_mfma_f32_32x32x16_bf16 v[80:95], v[140:143], v[200:203], v[80:95]\n"
      "v_mfma_f32_32x32x16_bf16 v[96:111], v[136:139], v[204:207], v[96:111]\n"
      "v_mfma_f32_32x32x16_bf16 v[112:127], v[140:143], v[204:207], v[112:127]\n"
      "s_mov_b32 s43, 1\n"
      "20:\n"
      "s_bitcmp1_b32 s40, 1\n"
      "s_cbranch_scc0 31f\n"
      "s_waitcnt vmcnt(8)\n"
      "v_max3_u32 %[t0], v208, v209, v210\n"
      "v_max3_u32 %[t1], v211, v212, v213\n"
      "v_max3_u32 %[t0], v214, v215, %[t0]\n"
      "v_max3_u32 %[t1], v216, v217, %[t1]\n"
      "v_max3_u32 %[t0], v218, v219, %[t0]\n"
      "v_max3_u32 %[t1], v220, v221, %[t1]\n"
      "v_max3_u32 %[t0], v222, v223, %[t0]\n"
      "v_max_u32 %[t0], %[t0], %[t1]\n"
      "v_cmp_eq_u32 vcc, -1, %[t0]\n"
      "s_cbranch_vccnz 21f\n"
      "s_bitset0_b32 s40, 1\n"
      "31:\n"
      "s_cmp_lg_u32 s43, 1\n"
      "s_cbranch_scc1 21f\n"
      "v_mfma_f32_32x32x16_bf16 v[64:79], v[144:147], v[208:211], v[64:79]\n"
      "v_mfma_f32_32x32x16_bf16 v[80:95], v[148:151], v[208:211], v[80:95]\n"
      "v_mfma_f32_32x32x16_bf16 v[96:111], v[144:147], v[212:215], v[96:111]\n"
      "v_mfma_f32_32x32x16_bf16 v[112:127], v[148:151], v[212:215], v[112:127]\n"
      "v_mfma_f32_32x32x16_bf16 v[64:79], v[152:155], v[216:219], v[64:79]\n"
      "v_mfma_f32_32x32x16_bf16 v[80:95], v[156:159], v[216:219], v[80:95]\n"
      "v_mfma_f32_32x32x16_bf16 v[96:111], v[152:155], v[220:223], v[96:111]\n"
      "v_mfma_f32_32x32x16_bf16 v[112:127], v[156:159], v[220:223], v[112:127]\n"
      "s_mov_b32 s43, 2\n"
      "21:\n"
      "s_bitcmp1_b32 s40, 2\n"
      "s_cbranch_scc0 32f\n"
      "s_waitcnt vmcnt(4)\n"
      "v_max3_u32 %[t0], v224, v225, v226\n"
      "v_max3_u32 %[t1], v227, v228, v229\n"
      "v_max3_u32 %[t0], v230, v231, %[t0]\n"
      "v_max3_u32 %[t1], v232, v233, %[t1]\n"
      "v_max3_u32 %[t0], v234, v235, %[t0]\n"
      "v_max3_u32 %[t1], v236, v237, %[t1]\n"
      "v_max3_u32 %[t0], v238, v239, %[t0]\n"
      "v_max_u32 %[t0], %[t0], %[t1]\n"
      "v_cmp_eq_u32 vcc, -1, %[t0]\n"
      "s_cbranch_vccnz 22f\n"
      "s_bitset0_b32 s40, 2\n"
      "32:\n"
      "s_cmp_lg_u32 s43, 2\n"
      "s_cbranch_scc1 22f\n"
      "v_mfma_f32_32x32x16_bf16 v[64:79], v[160:163], v[224:227], v[64:79]\n"
      "v_mfma_f32_32x32x16_bf16 v[80:95], v[164:167], v[224:227], v[80:95]\n"
      "v_mfma_f32_32x32x16_bf16 v[96:111], v[160:163], v[228:231], v[96:111]\n"
      "v_mfma_f32_32x32x16_bf16 v[112:127], v[164:167], v[228:231], v[112:127]\n"
      "v_mfma_f32_32x32x16_bf16 v[64:79], v[168:171], v[232:235], v[64:79]\n"
      "v_mfma_f32_32x32x16_bf16 v[80:95], v[172:175], v[232:235], v[80:95]\n"
      "v_mfma_f32_32x32x16_bf16 v[96:111], v[168:171], v[236:239], v[96:111]\n"
      "v_mfma_f32_32x32x16_bf16 v[112:127], v[172:175], v[236:239], v[112:127]\n"
      "s_mov_b32 s43, 3\n"
      "22:\n"
      "s_bitcmp1_b32 s40, 3\n"
      "s_cbranch_scc0 33f\n"
      "s_waitcnt vmcnt(0)\n"
      "v_max3_u32 %[t0], v240, v241, v242\n"
      "v_max3_u32 %[t1], v243, v244, v245\n"
      "v_max3_u32 %[t0], v246, v247, %[t0]\n"
      "v_max3_u32 %[t1], v248, v249, %[t1]\n"
      "v_max3_u32 %[t0], v250, v251, %[t0]\n"
      "v_max3_u32 %[t1], v252, v253, %[t1]\n"
      "v_max3_u32 %[t0], v254, v255, %[t0]\n"
      "v_max_u32 %[t0], %[t0], %[t1]\n"
      "v_cmp_eq_u32 vcc, -1, %[t0]\n"
      "s_cbranch_vccnz 23f\n"
      "s_bitset0_b32 s40, 3\n"
      "33:\n"
      "s_cmp_lg_u32 s43, 3\n"
      "s_cbranch_scc1 23f\n"
      "v_mfma_f32_32x32x16_bf16 v[64:79], v[176:179], v[240:243], v[64:79]\n"
      "v_mfma_f32_32x32x16_bf16 v[80:95], v[180:183], v[240:243], v[80:95]\n"
      "v_mfma_f32_32x32x16_bf16 v[96:111], v[176:179], v[244:247], v[96:111]\n"
      "v_mfma_f32_32x32x16_bf16 v[112:127], v[180:183], v[244:247], v[112:127]\n"
      "v_mfma_f32_32x32x16_bf16 v[64:79], v[184:187], v[248:251], v[64:79]\n"
      "v_mfma_f32_32x32x16_bf16 v[80:95], v[188:191], v[248:251], v[80:95]\n"
      "v_mfma_f32_32x32x16_bf16 v[96:111], v[184:187], v[252:255], v[96:111]\n"
      "v_mfma_f32_32x32x16_bf16 v[112:127], v[188:191], v[252:255], v[112:127]\n"
      "s_mov_b32 s43, 4\n"
      "23:\n"
      "s_cmp_eq_u32 s43, 4\n"
      "s_cbranch_scc1 3f\n"
      "s_sleep 0\n"
      "s_sub_u32 s41, s41, 1\n"
      "s_cbranch_scc0 1b\n"
      "3:\n"
      "s_nop 15\n"
          : "+{v[64:79]}"(acc[0]), "+{v[80:95]}"(acc[1]), "+{v[96:111]}"(acc[2]), "+{v[112:127]}"(acc[3]), [t0] "=&v"(t0), [t1] "=&v"(t1)
          : "{v[128:143]}"(wf[0]), "{v[144:159]}"(wf[1]), "{v[160:175]}"(wf[2]), "{v[176:191]}"(wf[3]), [o0] "v"(lane * 16 + mr * 32768), [o1] "v"(lane * 16 + (mr ^ 1) * 32768),
            [op0] "s"(op), [op1] "s"(op + 4096)
          : "v192", "v193", "v194", "v195", "v196", "v197", "v198", "v199", "v200", "v201", "v202", "v203", "v204", "v205", "v206", "v207", "v208", "v209", "v210", "v211", "v212", "v213", "v214", "v215", "v216", "v217", "v218", "v219", "v220", "v221", "v222", "v223", "v224", "v225", "v226", "v227", "v228", "v229", "v230", "v231", "v232", "v233", "v234", "v235", "v236", "v237", "v238", "v239", "v240", "v241", "v242", "v243", "v244", "v245", "v246", "v247", "v248", "v249", "v250", "v251", "v252", "v253", "v254", "v255", "s40", "s41", "s42", "s43", "vcc", "scc", "memory");
    }
#pragma unroll
    for (int k = 1; k < 4; k++)
#pragma unroll
      for (int g = 0; g < 4; g++)
        lds_wf4(((m ^ k) * 3 + k - 1) * 4096 + g * 1024 + lane * 16, (f32x4){acc[k][4 * g], acc[k][4 * g + 1], acc[k][4 * g + 2], acc[k][4 * g + 3]});
    BAR();
    unsigned o[8];
#pragma unroll
    for (int g = 0; g < 4; g++) {
      f32x4 s = {acc[0][4 * g], acc[0][4 * g + 1], acc[0][4 * g + 2], acc[0][4 * g + 3]};
#pragma unroll
      for (int k = 0; k < 3; k++) s += lds_f4((m * 3 + k) * 4096 + g * 1024 + lane * 16);
      o[2 * g] = pk2(s[0], s[1]); o[2 * g + 1] = pk2(s[2], s[3]);
    }
    u16* op2 = out + (size_t)(rt * 64 + mr * 32 + t) * 512 + ct * 64 + mc * 32 + 8 * h;
#pragma unroll
    for (int e = 0; e < 2; e++) {
      auto r0 = __builtin_amdgcn_permlane32_swap(o[4 * e], o[4 * e + 2], false, false);
      auto r1 = __builtin_amdgcn_permlane32_swap(o[4 * e + 1], o[4 * e + 3], false, false);
      *(u32x4*)(op2 + 16 * e) = (u32x4){r0[0], r1[0], r0[1], r1[1]};
    }
  }
}

extern "C" void kernel_launch(void* const* d_in, const int* in_sizes, int n_in, void* d_out, int out_size, void* d_ws, size_t ws_size,
                              hipStream_t stream) {
  (void)hipFuncSetAttribute((const void*)fused_block, hipFuncAttributeMaxDynamicSharedMemorySize, LDS_BYTES);
  fused_block<<<256, 512, LDS_BYTES, stream>>>((const u16*)d_in[0], (const u16*)d_in[1], (const u16*)d_in[2], (const u16*)d_in[4], (const u16*)d_in[5],
                                               (const u16*)d_in[6], (const u16*)d_in[7], (u16*)d_out, (char*)d_ws);
}
```
